```python
import torch

def _softmax(x, axis=-1):
    return torch.softmax(x, dim=axis)
def _gelu(x):
    return torch.nn.functional.gelu(x, approximate="tanh")
def _silu(x):
    return torch.nn.functional.silu(x)
def _sigmoid(x):
    return torch.sigmoid(x)
def _layernorm(x, axis=-1, eps=1e-5):
    m = x.mean(dim=axis, keepdim=True)
    v = x.var(dim=axis, keepdim=True, unbiased=False)
    return (x - m) / torch.sqrt(v + eps)
def _scatter_add(tgt, idx, src, axis=0):
    return tgt.clone().index_add_(axis, idx, src)
def _segment_sum(src, seg, n):
    out = torch.zeros((n,) + src.shape[1:], dtype=src.dtype)
    return out.index_add_(0, seg, src)
def _pad(x, pw):
    flat = tuple(v for pair in reversed(pw) for v in pair)
    return torch.nn.functional.pad(x, flat)

kernel_name = 'interleave_layers__sliding_local_dilated__dense_activation_mish__prompt'

def setup_inputs():

    _w = (lambda a: (lambda t: t.to(torch.bfloat16) if t.is_floating_point() else t)(torch.as_tensor(a)))
    def _A_setup():
        B, S, D = 1, 2048, 512
        return {
            "x": _w((lambda *s: torch.randn(*s, dtype=torch.bfloat16) * 0.05)(B, S, D)),
            "wqkv": _w((lambda *s: torch.randn(*s, dtype=torch.bfloat16) * 0.05)(D, 3 * D)),
            "wo": _w((lambda *s: torch.randn(*s, dtype=torch.bfloat16) * 0.05)(D, D)),
        }
    _A_in = _A_setup()

    def _B_setup():
        B, S, D, F = 1, 2048, 512, 1376
        return {
            "x": _w((lambda *s: torch.randn(*s, dtype=torch.bfloat16) * 0.05)(B, S, D)),
            "w1": _w((lambda *s: torch.randn(*s, dtype=torch.bfloat16) * 0.8)(D, F) * (D ** -0.5)),
            "b1": _w((lambda *s: torch.randn(*s, dtype=torch.bfloat16) * 0.8)(F)),
            "w2": _w((lambda *s: torch.randn(*s, dtype=torch.bfloat16) * 0.8)(F, D) * (F ** -0.5)),
        }
    _B_in = _B_setup()

    return {
        "A_x": _A_in["x"],
        "A_wqkv": _A_in["wqkv"],
        "A_wo": _A_in["wo"],
        "B_x": _B_in["x"],
        "B_w1": _B_in["w1"],
        "B_b1": _B_in["b1"],
        "B_w2": _B_in["w2"],
    }

def reference(A_x, A_wqkv, A_wo, B_x, B_w1, B_b1, B_w2):
    def _A_ref(x, wqkv, wo):
        B, S, D = x.shape
        H = 32; Dh = D // H
        W, dil = 8, 2
        qkv = (lambda x, *d: x.permute(*d))((x @ wqkv).reshape(B, S, 3, H, Dh), 2, 0, 3, 1, 4)
        q, k, v = qkv[0], qkv[1], qkv[2]
        scale = Dh ** -0.5
        pos = torch.arange(S)
        off = torch.arange(W) * dil
        idx = pos[:, None] - off[None, :]
        mask = idx >= 0
        idx_c = torch.where(mask, idx, 0).reshape(-1)
        kg = k[:, :, idx_c, :].reshape(B, H, S, W, Dh)
        vg = v[:, :, idx_c, :].reshape(B, H, S, W, Dh)
        logits = torch.einsum("bhsd,bhswd->bhsw", q, kg) * scale
        logits = torch.where(mask[None, None, :, :], logits, -1e9)
        attn = _softmax(logits, axis=-1)
        o = torch.einsum("bhsw,bhswd->bhsd", attn, vg)
        return (lambda x, *d: x.permute(*d))(o, 0, 2, 1, 3).reshape(B, S, D) @ wo

    def _B_ref(x, w1, b1, w2):
        h = torch.einsum("bsd,df->bsf", x, w1) + b1
        sp = torch.log1p(torch.exp(-torch.abs(h))) + torch.where(h > 0, h, torch.zeros_like(h))
        act = h * torch.tanh(sp)
        return torch.einsum("bsf,fd->bsd", act, w2)

    x = A_x
    x = x + _A_ref(_layernorm(x, axis=-1), A_wqkv, A_wo)
    x = x + _B_ref(_layernorm(x, axis=-1), B_w1, B_b1, B_w2)
    x = x + _A_ref(_layernorm(x, axis=-1), A_wqkv, A_wo)
    x = x + _B_ref(_layernorm(x, axis=-1), B_w1, B_b1, B_w2)
    return x
```

```cpp
#include <hip/hip_runtime.h>
#include <hip/hip_bf16.h>
#define WS_WQ 0x0u
#define WS_WO 0x180000u
#define WS_W1 0x200000u
#define WS_W2 0x380000u
#define WS_AUG 0x4e0000u
#define WS_FLG 0x1f10000u
#define WS_X0 0x500000u
#define WS_ST0 0x700000u
#define WS_PF 0x1f20000u
#define PREP_WT 0
#define PREP_CPF 1
#define PREP_TLBW 0
#define PREP_BFIRST 0
typedef unsigned short u16;
typedef unsigned u32x4 __attribute__((ext_vector_type(4)));
typedef float f32x2 __attribute__((ext_vector_type(2)));

__device__ __forceinline__ void st16(void* p, u32x4 v) { asm volatile("global_store_dwordx4 %0, %1, off sc0 sc1\n s_nop 1" :: "v"(p), "v"(v) : "memory"); }
__device__ __forceinline__ void st8(void* p, f32x2 v) { asm volatile("global_store_dwordx2 %0, %1, off sc0 sc1\n s_nop 1" :: "v"(p), "v"(v) : "memory"); }
__device__ __forceinline__ void st4(void* p, float v) { asm volatile("global_store_dword %0, %1, off sc0 sc1\n s_nop 1" :: "v"(p), "v"(v) : "memory"); }
__device__ __forceinline__ void st4u(void* p, unsigned v) { asm volatile("global_store_dword %0, %1, off sc0 sc1\n s_nop 1" :: "v"(p), "v"(v) : "memory"); }
__device__ __forceinline__ float bf2f(unsigned v) { return __builtin_bit_cast(float, v << 16); }
#define PST(k) do {} while (0)
#define WAITVM(n) asm volatile("s_waitcnt vmcnt(" #n ")" ::: "memory")

struct TileJob { int kind, cg, kt; };

__device__ __forceinline__ void tile_stage(u16* tile, int t, const u32x4 (&v)[4]) {
#pragma unroll
  for (int r = 0; r < 4; ++r) *(u32x4*)(tile + ((t >> 3) + 32 * r) * 72 + (t & 7) * 8) = v[r];
}
typedef unsigned u32x2 __attribute__((ext_vector_type(2)));
typedef __bf16 bf16x8 __attribute__((ext_vector_type(8)));
typedef float f32x4 __attribute__((ext_vector_type(4)));
#define TR8(r, a0, a1, a2, a3, s0, s1, s2, s3, H)                                                                                   \
  asm volatile("ds_read_b64_tr_b16 %0, %8 offset:" #s0 "\n ds_read_b64_tr_b16 %1, %8 offset:" #s0 "+" #H "\n"                       \
               "ds_read_b64_tr_b16 %2, %9 offset:" #s1 "\n ds_read_b64_tr_b16 %3, %9 offset:" #s1 "+" #H "\n"                       \
               "ds_read_b64_tr_b16 %4, %10 offset:" #s2 "\n ds_read_b64_tr_b16 %5, %10 offset:" #s2 "+" #H "\n"                     \
               "ds_read_b64_tr_b16 %6, %11 offset:" #s3 "\n ds_read_b64_tr_b16 %7, %11 offset:" #s3 "+" #H "\n"                     \
               "s_waitcnt lgkmcnt(0)"                                                                                               \
               : "=&v"(r[0]), "=&v"(r[1]), "=&v"(r[2]), "=&v"(r[3]), "=&v"(r[4]), "=&v"(r[5]), "=&v"(r[6]), "=&v"(r[7])             \
               : "v"(a0), "v"(a1), "v"(a2), "v"(a3) : "memory")
template <bool DEFER, int SIDE = 0>
__device__ __forceinline__ void tile_out16(const TileJob& J, unsigned tile, int t, float bias, unsigned char* ws, u32x4 (&o)[4], unsigned (&off)[4], float& cs0) {
  const int lane = t & 63, wv = t >> 6, kind = J.kind, cg = J.cg, kt = J.kt;
  const unsigned ad = tile + (8 * (lane >> 4) + ((lane & 15) >> 2)) * 144 + (16 * wv + 4 * (lane & 3)) * 2;
  u32x2 r[8];
  if (SIDE != 2) {
  TR8(r, ad, ad, ad, ad, 0, 4608, 9216, 13824, 576);
#pragma unroll
  for (int sl = 0; sl < 4; ++sl) { o[sl][0] = r[2 * sl][0]; o[sl][1] = r[2 * sl][1]; o[sl][2] = r[2 * sl + 1][0]; o[sl][3] = r[2 * sl + 1][1]; }
  const u32x4 ones = {0x3f803f80u, 0x3f803f80u, 0x3f803f80u, 0x3f803f80u};
  f32x4 cs = {0.f, 0.f, 0.f, 0.f};
#pragma unroll
  for (int sl = 0; sl < 4; ++sl)
    cs = __builtin_amdgcn_mfma_f32_16x16x32_bf16(__builtin_bit_cast(bf16x8, ones), __builtin_bit_cast(bf16x8, o[sl]), cs, 0, 0, 0);
  cs0 = cs[0];
  }
  size_t base;
  int hw, tile3;
  bool dummy = false;
  if (kind == 0) {
    tile3 = cg >> 3; hw = 4 * (cg & 7) + wv;
    base = WS_WQ + (size_t)hw * 49152 + (12 * kt + tile3) * 1024;
  } else {
    const int ft = 4 * cg + wv, j = ft / 11, rem = ft - 11 * j, w = rem / 3;
    tile3 = rem - 3 * w; hw = 4 * j + w;
    base = WS_W1 + (size_t)hw * 49152 + (12 * kt + tile3) * 1024;
    dummy = rem == 10;
  }
  if (SIDE != 1 && dummy) {
    const u32x4 z = {0u, 0u, 0u, 0u};
#pragma unroll
    for (int sl = 0; sl < 4; ++sl) st16(ws + base + sl * 3072 + 1024 + lane * 16, z);
  }
  if (SIDE != 1 && lane < 16) {
    float* a = (float*)(ws + WS_AUG + (size_t)(((kind >> 1) * 32 + hw) * 3 + tile3) * 512);
    st4(a + lane * 4 + kt, cs0);
    st4(a + 64 + lane * 4 + kt, bias);
    if (dummy) { st4(a + 128 + lane * 4 + kt, 0.f); st4(a + 192 + lane * 4 + kt, 0.f); }
  }
#pragma unroll
  for (int sl = 0; sl < 4; ++sl) {
    if (SIDE == 2) break;
    if (DEFER) off[sl] = (unsigned)base + sl * 3072 + lane * 16;
    else st16(ws + base + sl * 3072 + lane * 16, o[sl]);
  }
}
__device__ __forceinline__ void tile_out32(const TileJob& J, unsigned tile, int t, u32x4 (&o)[4], unsigned (&off)[4]) {
  const int lane = t & 63, wv = t >> 6, kind = J.kind, cg = J.cg, kt = J.kt;
  const int G = lane >> 4, rr = (lane & 15) >> 2, c = lane & 3;
  unsigned ad[4];
#pragma unroll
  for (int q = 0; q < 4; ++q) {
    const int sl = 2 * wv + (q >> 1), tt = q & 1;
    const int s = 8 * kt + sl;
    int w, i;
    if (kind == 1) { w = s >> 3; i = s & 7; } else { w = s / 22; i = s - 22 * w; }
    ad[q] = tile + (16 * sl + 8 * (G >> 1) + rr) * 144 + (16 * ((w + 2 * tt + (G & 1)) & 3) + 8 * (c & 1) + 4 * (c >> 1)) * 2;
    off[q] = ((kind == 1) ? (WS_WO + (4 * cg + w) * 16384 + (2 * i + tt) * 1024)
                          : (WS_W2 + (4 * cg + w) * 45056 + (2 * i + tt) * 1024)) + lane * 16;
  }
  u32x2 r[8];
  TR8(r, ad[0], ad[1], ad[2], ad[3], 0, 0, 0, 0, 576);
#pragma unroll
  for (int q = 0; q < 4; ++q) {
    o[q][0] = r[2 * q][0]; o[q][1] = r[2 * q][1]; o[q][2] = r[2 * q + 1][0]; o[q][3] = r[2 * q + 1][1];
  }
}

__device__ __forceinline__ unsigned prep_part(const u16* x, const u16* wqkv, const u16* wo, const u16* w1, const u16* w2, const u16* b1,
                                              unsigned char* ws, unsigned char* lds, unsigned& offB,
                                              u32x4 (&ob)[4], unsigned (&fb)[4], const unsigned short* out) {
  const int t = threadIdx.x, wv = t >> 6, b = blockIdx.x;
  PST(0);
  unsigned pf0, pf1;
#define CODE_TOUCH()                                                                                                                  \
  {                                                                                                                                   \
    unsigned long long pc; unsigned len;                                                                                              \
    asm volatile("s_getpc_b64 %0\nLCPF_%=:\n s_mov_b32 %1, MAINEND-LCPF_%=-4" : "=s"(pc), "=s"(len));                                 \
    const unsigned h_ = (PREP_CPF & 8) ? ((blockIdx.x >> 3) & 1u) * 32768u : 0u;                                                      \
    const unsigned o0 = min((unsigned)threadIdx.x * 128u + h_, len) & ~3u, o1 = min((unsigned)threadIdx.x * 128u + 32768u, len) & ~3u; \
    asm volatile("global_load_dword %0, %1, %2" : "=v"(pf0) : "v"(o0), "s"(pc));                                                      \
    if (!(PREP_CPF & 8)) asm volatile("global_load_dword %0, %1, %2" : "=v"(pf1) : "v"(o1), "s"(pc));                                 \
    else pf1 = 0;                                                                                                                     \
  }
#if !(PREP_CPF & 4)
  CODE_TOUCH();
#endif
  const unsigned epoch = __hip_atomic_load((const unsigned*)(ws + WS_PF), __ATOMIC_RELAXED, __HIP_MEMORY_SCOPE_AGENT);
  u16* tileA = (u16*)lds;
  u16* tileB = (u16*)(lds + 20480);
  const unsigned ldsa = (unsigned)(size_t)lds;
  const bool ax = b < 128, aw = b >= 128 && b < 224, hb = b >= 48;
  const int xi = t & 15, sg = t >> 4, xg = (b & 127) >> 1, xblk = b & 1;
  TileJob JA, JB;
  JA.kind = 0; JA.cg = (b - 128) >> 2; JA.kt = b & 3;
  u32x4 va[4], vb[4];
  {
    const int tok_ = 64 * (xg >> 1) + 2 * (16 * xblk + xi) + (xg & 1);
    const int cgA = aw ? JA.cg : 0;
    const u16* pa = ax ? x + (size_t)tok_ * 512 + sg * 32 : wqkv + (size_t)(JA.kt * 128 + (t >> 3)) * 1536 + cgA * 64 + (t & 7) * 8;
    const int sa = ax ? 8 : 32 * 1536;
#pragma unroll
    for (int q = 0; q < 4; ++q) va[q] = *(const u32x4*)(pa + (size_t)q * sa);
    asm volatile("" ::: "memory");
    __builtin_amdgcn_sched_barrier(0);
  }
  const int jb = hb ? 255 - b : 0;
  const bool k1 = jb < 32, k2 = !k1 && jb < 120;
  const int j3 = jb - 120, q3 = (j3 * 745) >> 13;
  JB.kind = k1 ? 1 : k2 ? 2 : 3;
  JB.cg = k1 ? jb >> 2 : k2 ? (jb - 32) >> 2 : q3;
  JB.kt = (k1 || k2) ? jb & 3 : j3 - 11 * q3;
  const u16* WB = k1 ? wo : k2 ? w1 : w2;
  const int ldB = k2 ? 1376 : 512, nrB = (k1 || k2) ? 512 : 1376, ncB = k2 ? 1376 : 512;
  const int colb = JB.cg * 64 + 16 * (t >> 6) + (t & 15);
  unsigned braw;
  {
    const int colB = JB.cg * 64 + (t & 7) * 8, colc = min(colB, ncB - 8);
#pragma unroll
    for (int r = 0; r < 4; ++r) {
      const int row = JB.kt * 128 + (t >> 3) + 32 * r;
      vb[r] = *(const u32x4*)(WB + (size_t)min(row, nrB - 1) * ldB + colc);
    }
    braw = b1[min(colb, 1375)];
    asm volatile("" ::: "memory");
  }
  PST(1);
  const unsigned tok = __builtin_amdgcn_readfirstlane(epoch) + 1u;
  if (aw) {
    tile_stage(tileA, t, va);
    __syncthreads();
  }
  PST(2);
  if (ax) {
    asm volatile("" : "+v"(va[0]), "+v"(va[1]), "+v"(va[2]), "+v"(va[3]));
    unsigned char* dst = ws + WS_X0 + xg * 32768 + xblk * 16384 + sg * 1024 + xi * 16;
#pragma unroll
    for (int q = 0; q < 4; ++q) st16(dst + q * 256, va[q]);
#pragma unroll
    for (int sl = 0; sl < 2; ++sl) {
      float s1 = 0.f, s2 = 0.f;
#pragma unroll
      for (int q = 0; q < 2; ++q)
#pragma unroll
        for (int e = 0; e < 4; ++e) {
          const unsigned u = va[2 * sl + q][e];
          const float a = bf2f(u & 0xffffu), c = bf2f(u >> 16);
          s1 += a + c; s2 += a * a + c * c;
        }
      f32x2 o = {s1, s2};
      st8(ws + WS_ST0 + xg * 8192 + xblk * 4096 + (2 * sg + sl) * 128 + xi * 8, o);
    }
  } else if (aw) {
    u32x4 oa[4]; unsigned fa[4];
    float c0_; tile_out16<false>(JA, ldsa, t, 0.f, ws, oa, fa, c0_);
  } else {
    asm volatile("" :: "v"(va[0]), "v"(va[1]), "v"(va[2]), "v"(va[3]));
  }
  PST(3);
  WAITVM(0);
  asm volatile("" :: "v"(pf0), "v"(pf1));
  asm volatile("" : "+v"(vb[0]), "+v"(vb[1]), "+v"(vb[2]), "+v"(vb[3]), "+v"(braw));
  __syncthreads();
  if (t == 192) {
    unsigned char* pf = ws + WS_PF;
    st4u(pf + (ax ? 0x1000 + (xg >> 1) * 128 + ((xg & 1) * 2 + xblk) * 4 : aw ? 0x2000 + (JA.cg & 7) * 128 + ((JA.cg >> 3) * 4 + JA.kt) * 4 : 0x8400), tok);
    st4u(pf + 0x4000 + b * 4, tok);
  }
  PST(4);
#pragma unroll
  for (int r = 0; r < 4; ++r) {
    const bool ok = JB.kt * 128 + (t >> 3) + 32 * r < nrB && JB.cg * 64 + (t & 7) * 8 < ncB;
#pragma unroll
    for (int e = 0; e < 4; ++e) vb[r][e] = ok ? vb[r][e] : 0u;
  }
  tile_stage(tileB, t, vb);
  const float bias = (JB.kind == 2 && JB.kt == 0 && colb < 1376) ? bf2f(braw) : 0.f;
  __syncthreads();
  float csB = 0.f;
  if (JB.kind == 2 && hb) tile_out16<true, PREP_BFIRST ? 1 : 0>(JB, ldsa + 20480, t, bias, ws, ob, fb, csB);
  else tile_out32(JB, ldsa + 20480, t, ob, fb);
  if (!hb) {
#pragma unroll
    for (int q = 0; q < 4; ++q) fb[q] = 0x40000000u;
  }
  offB = WS_PF + (hb ? 0x3000 + jb * 4 : 0x8480);
  return tok;
}

__global__ __launch_bounds__(256, 1) void main_kernel(const u16* x, const u16* wqkv, const u16* wo,
    const u16* w1, const u16* w2, const u16* b1, unsigned char* ws, unsigned short* out) {
  __shared__ __attribute__((aligned(1024))) unsigned char lds[163840];
  unsigned offB, fb[4];
  u32x4 ob[4];
  const unsigned tok = prep_part(x, wqkv, wo, w1, w2, b1, ws, lds, offB, ob, fb, out);
  const unsigned ldsb = (unsigned)(size_t)lds;
  asm volatile(
    "MAINCODE:\n"
    "s_load_dwordx2 s[16:17], %[ka], 0x30\n"
    "s_load_dwordx2 s[20:21], %[ka], 0x38\n"
    "s_waitcnt lgkmcnt(0)\n"
    "s_mov_b32 s18, 0x1f30000\n"
    "s_mov_b32 s19, 0x20000\n"
    "s_mov_b32 s22, 0x200000\n"
    "s_mov_b32 s23, 0x20000\n"
    "s_mov_b32 s86, %[ldsb]\n"
    "s_mov_b32 s66, %[bid]\n"
    "s_mov_b32 s98, %[tok]\n"
    "v_lshrrev_b32 v125, 6, %[tid]\n"
    "v_and_b32 v120, 63, %[tid]\n"
    "v_readfirstlane_b32 s28, v125\n"
    "s_and_b32 s63, s66, 7\n"
    "s_lshr_b32 s64, s66, 3\n"
    "s_and_b32 s29, s63, 1\n"
    "s_lshl_b32 s29, s29, 2\n"
    "s_and_b32 s65, s64, 3\n"
    "s_add_u32 s29, s29, s65\n"
    "s_lshr_b32 s63, s63, 1\n"
    "s_lshl_b32 s63, s63, 3\n"
    "s_lshr_b32 s64, s64, 2\n"
    "s_add_u32 s30, s63, s64\n"
    "v_and_b32 v121, 15, v120\n"
    "v_lshrrev_b32 v122, 4, v120\n"
    "v_lshrrev_b32 v123, 5, v120\n"
    "v_and_b32 v124, 31, v120\n"
    "v_lshlrev_b32 v1, 4, v120\n"
    "s_lshl_b32 s92, s30, 1\n"
    "s_cmp_eq_u32 s30, 0\n"
    "s_cselect_b32 s63, 0, 2\n"
    "s_sub_u32 s93, s92, s63\n"
    "s_lshl_b32 s94, s29, 2\n"
    "s_add_u32 s94, s94, s28\n"
    "s_mul_i32 s63, s92, 0x8000\n"
    "s_mul_i32 s64, s28, 0x800\n"
    "s_add_u32 s31, s63, s64\n"
    "s_mul_i32 s65, s93, 0x8000\n"
    "s_add_u32 s95, s65, s64\n"
    "s_add_u32 s95, s95, 0x4000\n"
    "s_mul_i32 s48, s28, 0x400\n"
    "s_add_u32 s48, s48, s86\n"
    "s_mul_i32 s49, s28, 0x800\n"
    "s_add_u32 s49, s49, s86\n"
    "s_mul_i32 s50, s28, 0x1000\n"
    "s_add_u32 s50, s50, s86\n"
    "v_lshlrev_b32 v125, 10, v123\n"
    "v_lshrrev_b32 v126, 3, v120\n"
    "v_and_b32 v126, 3, v126\n"
    "v_lshlrev_b32 v126, 8, v126\n"
    "v_add_u32 v148, v125, v126\n"
    "v_and_b32 v126, 7, v120\n"
    "v_lshlrev_b32 v126, 4, v126\n"
    "v_add_u32 v148, v148, v126\n"
    "v_add_u32 v148, 0x80, v148\n"
    "s_mul_i32 s65, s94, 0xc000\n"
    "s_add_u32 s38, s65, 0x1000\n"
    "s_add_u32 s40, s65, 0x201000\n"
    "s_mov_b32 s67, 0x20000\n"
    "s_lshl_b32 s63, s29, 7\n"
    "s_add_u32 s63, s63, 0x1f22000\n"
    "s_lshl_b32 s64, s30, 7\n"
    "s_add_u32 s64, s64, 0x1f20fd0\n"
    "s_max_u32 s65, s30, 1\n"
    "s_lshl_b32 s65, s65, 7\n"
    "s_add_u32 s65, s65, 0x1f20f40\n"
    "v_lshlrev_b32 v125, 2, v120\n"
    "v_add_u32 v144, s63, v125\n"
    "v_add_u32 v126, s64, v125\n"
    "v_cmp_lt_u32 vcc, 11, v120\n"
    "s_nop 1\n"
    "v_cndmask_b32 v144, v144, v126, vcc\n"
    "v_add_u32 v126, s65, v125\n"
    "v_cmp_lt_u32 vcc, 15, v120\n"
    "s_nop 1\n"
    "v_cndmask_b32 v144, v144, v126, vcc\n"
    "s_mul_i32 s63, s92, 0x8000\n"
    "s_mul_i32 s64, s28, 0x800\n"
    "s_mul_i32 s65, s28, 0x1000\n"
    "s_add_u32 s96, s63, s65\n"
    "s_add_u32 s96, s96, 0x4000\n"
    "s_mul_i32 s65, s28, 0x2000\n"
    "s_add_u32 s36, s63, s65\n"
    "s_mul_i32 s65, s94, 0x400\n"
    "s_add_u32 s44, s63, s65\n"
    "s_mul_i32 s65, s94, 0x200\n"
    "s_add_u32 s42, s63, s65\n"
    "s_mul_i32 s63, s92, 0x2000\n"
    "s_add_u32 s35, s63, s64\n"
    "s_mul_i32 s65, s94, 0x80\n"
    "s_add_u32 s43, s63, s65\n"
    "s_mul_i32 s63, s93, 0x2000\n"
    "s_mul_i32 s65, s28, 0x400\n"
    "s_add_u32 s34, s63, s65\n"
    "s_add_u32 s34, s34, 0x1000\n"
    "s_mul_i32 s63, s92, 0x16000\n"
    "s_mul_i32 s65, s28, 0x5800\n"
    "s_add_u32 s37, s63, s65\n"
    "s_mul_i32 s65, s29, 0x2c00\n"
    "s_mul_i32 s64, s28, 0xc00\n"
    "s_add_u32 s45, s63, s65\n"
    "s_add_u32 s45, s45, s64\n"
    "s_mul_i32 s63, s94, 0x4000\n"
    "s_add_u32 s39, s63, 0x181000\n"
    "s_mul_i32 s63, s94, 0xb000\n"
    "s_add_u32 s41, s63, 0x381000\n"
    "s_mul_i32 s63, s30, 0x10000\n"
    "s_mul_i32 s64, s94, 32\n"
    "s_add_u32 s46, s63, s64\n"
    "s_mul_i32 s63, s92, 0x80\n"
    "s_mul_i32 s64, s29, 4\n"
    "s_add_u32 s47, s63, s64\n"
    "s_mul_i32 s51, s28, 0x2000\n"
    "s_add_u32 s51, s51, s86\n"
    "s_mul_i32 s52, s28, 0x4400\n"
    "s_add_u32 s52, s52, s86\n"
    "s_mul_i32 s53, s28, 0x5800\n"
    "s_add_u32 s53, s53, s86\n"
    "s_sub_u32 s63, s28, 1\n"
    "s_and_b32 s63, s63, 3\n"
    "s_mul_i32 s54, s63, 0x2000\n"
    "s_add_u32 s54, s54, s86\n"
    "s_mul_i32 s57, s63, 0x5800\n"
    "s_add_u32 s57, s57, s86\n"
    "s_mul_i32 s60, s63, 0x4400\n"
    "s_add_u32 s60, s60, s86\n"
    "s_sub_u32 s63, s28, 2\n"
    "s_and_b32 s63, s63, 3\n"
    "s_mul_i32 s55, s63, 0x2000\n"
    "s_add_u32 s55, s55, s86\n"
    "s_mul_i32 s58, s63, 0x5800\n"
    "s_add_u32 s58, s58, s86\n"
    "s_mul_i32 s61, s63, 0x4400\n"
    "s_add_u32 s61, s61, s86\n"
    "s_sub_u32 s63, s28, 3\n"
    "s_and_b32 s63, s63, 3\n"
    "s_mul_i32 s56, s63, 0x2000\n"
    "s_add_u32 s56, s56, s86\n"
    "s_mul_i32 s59, s63, 0x5800\n"
    "s_add_u32 s59, s59, s86\n"
    "s_mul_i32 s62, s63, 0x4400\n"
    "s_add_u32 s62, s62, s86\n"
    "s_min_u32 s63, s28, 2\n"
    "s_mul_i32 s70, s63, 0x1000\n"
    "s_add_u32 s70, s70, s86\n"
    "s_mul_i32 s72, s63, 0x200\n"
    "s_mul_i32 s74, s63, 64\n"
    "s_and_b32 s63, s28, 1\n"
    "s_mul_i32 s71, s63, 0x1000\n"
    "s_add_u32 s71, s71, s86\n"
    "s_add_u32 s63, s63, 1\n"
    "s_mul_i32 s73, s63, 0x200\n"
    "s_mul_i32 s75, s63, 64\n"
    "s_cmp_eq_u32 s28, 0\n"
    "s_cselect_b32 s68, 1, 0\n"
    "s_mov_b32 s69, 0\n"
    "s_mov_b32 s76, 0x3eb8aa3b\n"
    "s_mov_b32 s77, 0x3f317218\n"
    "s_mov_b32 s78, 0x3fb8aa3b\n"
    "s_mov_b32 s80, 0x3b000000\n"
    "s_mov_b32 s79, 0xbb000000\n"
    "s_mov_b32 s81, 0x3727c5ac\n"
    "s_cmp_eq_u32 s28, 3\n"
    "s_cselect_b32 s63, 0x40000000, 0\n"
    "v_add_u32 v151, s63, v1\n"
    "v_add_u32 v2, s86, v1\n"
    "v_add_u32 v3, 0xc000, v2\n"
    "v_add_u32 v4, 0x18000, v2\n"
    "v_add_u32 v6, s53, v1\n"
    "s_add_u32 s63, s52, 0x16000\n"
    "v_add_u32 v7, s63, v1\n"
    "v_and_b32 v125, 1, v122\n"
    "v_lshlrev_b32 v126, 14, v125\n"
    "v_lshlrev_b32 v127, 8, v123\n"
    "v_add_u32 v8, v126, v127\n"
    "s_mov_b64 exec, 1048575\n"
    "buffer_load_dword v60, v144, s[16:19], 0 offen sc1\n"
    "s_mov_b64 exec, -1\n"
    "v_lshlrev_b32 v126, 4, v121\n"
    "v_add_u32 v8, v8, v126\n"
    "v_lshlrev_b32 v126, 12, v125\n"
    "v_lshlrev_b32 v127, 3, v121\n"
    "v_add_u32 v9, v126, v127\n"
    "v_lshlrev_b32 v126, 2, v123\n"
    "v_add_u32 v9, v9, v126\n"
    "v_lshlrev_b32 v126, 7, v122\n"
    "v_add_u32 v12, v126, v127\n"
    "v_and_b32 v127, 7, v121\n"
    "v_lshlrev_b32 v127, 4, v127\n"
    "v_add_u32 v149, v126, v127\n"
    "v_add_u32 v149, s86, v149\n"
    "v_lshlrev_b32 v126, 11, v124\n"
    "v_lshlrev_b32 v127, 4, v123\n"
    "v_add_u32 v10, v126, v127\n"
    "s_mul_i32 s63, s92, 0x80\n"
    "s_mul_i32 s64, s93, 0x80\n"
    "v_and_b32 v126, 7, v120\n"
    "v_lshlrev_b32 v126, 2, v126\n"
    "v_add_u32 v11, s63, v126\n"
    "v_add_u32 v127, s64, v126\n"
    "v_cmp_lt_u32 vcc, 7, v120\n"
    "s_nop 1\n"
    "v_cndmask_b32 v11, v11, v127, vcc\n"
    "s_add_u32 s63, s86, 0x27000\n"
    "v_lshlrev_b32 v150, 5, v121\n"
    "v_add_u32 v150, s63, v150\n"
    "v_min_u32 v126, 2, v122\n"
    "v_lshlrev_b32 v126, 3, v126\n"
    "v_add_u32 v15, v150, v126\n"
    "v_lshlrev_b32 v13, 4, v122\n"
    "v_add_u32 v13, s63, v13\n"
    "v_lshlrev_b32 v14, 2, v121\n"
    "v_add_u32 v14, s63, v14\n"
    "v_mov_b32 v128, 0\n"
    "v_mov_b32 v129, 0\n"
    "v_mov_b32 v130, 0\n"
    "v_mov_b32 v131, 0\n"
    "s_lshl_b32 s64, s28, 10\n"
    "s_add_u32 s64, s64, s63\n"
    "v_add_u32 v126, s64, v1\n"
    "ds_write_b128 v126, v[128:131] offset:0\n"
    "v_lshlrev_b32 v125, 2, v122\n"
    "s_mov_b32 s63, 0xf149f2ca\n"
    "v_mov_b32 v132, s63\n"
    "s_cmp_eq_u32 s30, 0\n"
    "s_cselect_b64 s[90:91], -1, 0\n"
    "v_add_u32 v126, 0, v125\n"
    "v_sub_u32 v127, v121, v126\n"
    "v_cmp_gt_u32 vcc, 8, v127\n"
    "s_nop 1\n"
    "v_cndmask_b32 v40, v132, 0, vcc\n"
    "v_sub_u32 v127, v126, v121\n"
    "v_cmp_le_i32 vcc, 9, v127\n"
    "s_nop 1\n"
    "v_cndmask_b32 v36, v132, 0, vcc\n"
    "v_cndmask_b32 v32, v36, v132, s[90:91]\n"
    "v_add_u32 v126, 1, v125\n"
    "v_sub_u32 v127, v121, v126\n"
    "v_cmp_gt_u32 vcc, 8, v127\n"
    "s_nop 1\n"
    "v_cndmask_b32 v41, v132, 0, vcc\n"
    "v_sub_u32 v127, v126, v121\n"
    "v_cmp_le_i32 vcc, 9, v127\n"
    "s_nop 1\n"
    "v_cndmask_b32 v37, v132, 0, vcc\n"
    "v_cndmask_b32 v33, v37, v132, s[90:91]\n"
    "v_add_u32 v126, 2, v125\n"
    "v_sub_u32 v127, v121, v126\n"
    "v_cmp_gt_u32 vcc, 8, v127\n"
    "s_nop 1\n"
    "v_cndmask_b32 v42, v132, 0, vcc\n"
    "v_sub_u32 v127, v126, v121\n"
    "v_cmp_le_i32 vcc, 9, v127\n"
    "s_nop 1\n"
    "v_cndmask_b32 v38, v132, 0, vcc\n"
    "v_cndmask_b32 v34, v38, v132, s[90:91]\n"
    "v_add_u32 v126, 3, v125\n"
    "v_sub_u32 v127, v121, v126\n"
    "v_cmp_gt_u32 vcc, 8, v127\n"
    "s_nop 1\n"
    "v_cndmask_b32 v43, v132, 0, vcc\n"
    "v_sub_u32 v127, v126, v121\n"
    "v_cmp_le_i32 vcc, 9, v127\n"
    "s_nop 1\n"
    "v_cndmask_b32 v39, v132, 0, vcc\n"
    "v_cndmask_b32 v35, v39, v132, s[90:91]\n"
    "v_mov_b32 v44, 0x3f803f80\n"
    "v_mov_b32 v45, 0x3f803f80\n"
    "v_mov_b32 v46, 0x3f803f80\n"
    "v_mov_b32 v47, 0x3f803f80\n"
    "v_lshlrev_b32 v125, 2, v120\n"
    "s_lshl_b32 s63, s29, 4\n"
    "s_add_u32 s63, s63, 0x1f23000\n"
    "s_mul_i32 s64, s29, 11\n"
    "s_lshr_b32 s65, s64, 2\n"
    "s_lshl_b32 s64, s64, 2\n"
    "s_add_u32 s64, s64, 0x1f231d0\n"
    "s_lshl_b32 s65, s65, 4\n"
    "s_add_u32 s65, s65, 0x1f23044\n"
    "v_add_u32 v153, s63, v125\n"
    "v_add_u32 v126, s64, v125\n"
    "v_cmp_lt_u32 vcc, 3, v120\n"
    "s_nop 1\n"
    "v_cndmask_b32 v153, v153, v126, vcc\n"
    "v_add_u32 v126, s65, v125\n"
    "v_min_u32 v126, 0x1f231dc, v126\n"
    "v_cmp_lt_u32 vcc, 14, v120\n"
    "s_nop 1\n"
    "v_cndmask_b32 v153, v153, v126, vcc\n"
    "s_cmp_eq_u32 s28, 0\n"
    "s_cbranch_scc0 .LAend\n"
    "s_mov_b64 exec, 1048575\n"
    "s_branch .LAw\n"
    ".LAs:\n"
    "buffer_load_dword v60, v144, s[16:19], 0 offen sc1\n"
    ".LAw:\n"
    "s_waitcnt vmcnt(0)\n"
    "v_cmp_ne_u32 vcc, s98, v60\n"
    "s_nop 1\n"
    "s_cbranch_vccz .LAe\n"
    "s_sub_u32 s67, s67, 1\n"
    "s_cbranch_scc0 .LAs\n"
    "s_mov_b32 s67, 0\n"
    ".LAe:\n"
    "s_mov_b64 exec, -1\n"
    ".LAend:\n"
    "s_barrier\n"
    "s_add_u32 m0, s48, 0x1400\n"
    "s_add_u32 s87, s95, 0x500000\n"
    "v_add_u32 v62, s87, v148\n"
    "global_load_lds_dwordx4 v62, s[16:17] sc1\n"
    "s_add_u32 m0, s49, 0x6c00\n"
    "s_add_u32 s87, s31, 0x500000\n"
    "v_add_u32 v62, s87, v1\n"
    "global_load_lds_dwordx4 v62, s[16:17] sc1\n"
    "global_load_lds_dwordx4 v62, s[16:17] offset:1024 sc1\n"
    "v_add_u32 v61, s38, v1\n"
    "global_load_dwordx4 a[0:3], v61, s[16:17] offset:-4096\n"
    "global_load_dwordx4 a[4:7], v61, s[16:17] offset:-3072\n"
    "global_load_dwordx4 a[8:11], v61, s[16:17] offset:-2048\n"
    "global_load_dwordx4 a[12:15], v61, s[16:17] offset:-1024\n"
    "global_load_dwordx4 a[16:19], v61, s[16:17]\n"
    "global_load_dwordx4 a[20:23], v61, s[16:17] offset:1024\n"
    "global_load_dwordx4 a[24:27], v61, s[16:17] offset:2048\n"
    "global_load_dwordx4 a[28:31], v61, s[16:17] offset:3072\n"
    "v_add_u32 v61, 0x2000, v61\n"
    "global_load_dwordx4 a[32:35], v61, s[16:17] offset:-4096\n"
    "global_load_dwordx4 a[36:39], v61, s[16:17] offset:-3072\n"
    "global_load_dwordx4 a[40:43], v61, s[16:17] offset:-2048\n"
    "global_load_dwordx4 a[44:47], v61, s[16:17] offset:-1024\n"
    "global_load_dwordx4 a[48:51], v61, s[16:17]\n"
    "global_load_dwordx4 a[52:55], v61, s[16:17] offset:1024\n"
    "global_load_dwordx4 a[56:59], v61, s[16:17] offset:2048\n"
    "global_load_dwordx4 a[60:63], v61, s[16:17] offset:3072\n"
    "v_add_u32 v61, 0x2000, v61\n"
    "global_load_dwordx4 a[64:67], v61, s[16:17] offset:-4096\n"
    "global_load_dwordx4 a[68:71], v61, s[16:17] offset:-3072\n"
    "s_mul_i32 s88, s94, 0x600\n"
    "s_add_u32 s88, s88, 0x4e0000\n"
    "v_add_u32 v188, s88, v1\n"
    "v_mov_b32 v156, 0\n"
    "v_mov_b32 v157, 0\n"
    "v_mov_b32 v158, 0\n"
    "v_mov_b32 v159, 0\n"
    "v_mov_b32 v160, 0\n"
    "v_mov_b32 v161, 0\n"
    "v_mov_b32 v162, 0\n"
    "v_mov_b32 v163, 0\n"
    "v_mov_b32 v164, 0\n"
    "v_mov_b32 v165, 0\n"
    "v_mov_b32 v166, 0\n"
    "v_mov_b32 v167, 0\n"
    "v_mov_b32 v168, 0\n"
    "v_mov_b32 v169, 0\n"
    "v_mov_b32 v170, 0\n"
    "v_mov_b32 v171, 0\n"
    "v_mov_b32 v172, 0\n"
    "v_mov_b32 v173, 0\n"
    "v_mov_b32 v174, 0\n"
    "v_mov_b32 v175, 0\n"
    "v_mov_b32 v176, 0\n"
    "v_mov_b32 v177, 0\n"
    "v_mov_b32 v178, 0\n"
    "v_mov_b32 v179, 0\n"
    "s_waitcnt lgkmcnt(0)\n"
    "s_waitcnt vmcnt(17)\n"
    "s_barrier\n"
    "v_add_u32 v63, 0x1400, v149\n"
    "ds_read_b128 v[96:99], v63 offset:0\n"
    "ds_read_b128 v[100:103], v2 offset:27648\n"
    "ds_read_b128 v[104:107], v63 offset:512\n"
    "ds_read_b128 v[108:111], v2 offset:28672\n"
    "ds_read_b128 v[112:115], v63 offset:1024\n"
    "ds_read_b128 v[116:119], v2 offset:29696\n"
    "s_waitcnt vmcnt(16)\n"
    "s_waitcnt lgkmcnt(4)\n"
    "v_mfma_f32_16x16x32_bf16 v[64:67], a[4:7], v[96:99], 0\n"
    "global_load_dwordx4 a[72:75], v61, s[16:17] offset:-2048\n"
    "global_load_dwordx4 a[76:79], v61, s[16:17] offset:-1024\n"
    "v_mfma_f32_16x16x32_bf16 v[72:75], a[0:3], v[100:103], 0\n"
    "s_waitcnt vmcnt(17)\n"
    "v_mfma_f32_16x16x32_bf16 v[68:71], v[96:99], a[8:11], 0\n"
    "global_load_dwordx4 a[80:83], v61, s[16:17]\n"
    "v_mfma_f32_16x16x32_bf16 v[76:79], a[4:7], v[100:103], 0\n"
    "v_mfma_f32_16x16x32_bf16 v[80:83], v[100:103], a[8:11], 0\n"
    "ds_read_b128 v[96:99], v63 offset:1536\n"
    "ds_read_b128 v[100:103], v2 offset:30720\n"
    "s_waitcnt vmcnt(16)\n"
    "s_waitcnt lgkmcnt(2)\n"
    "v_mfma_f32_16x16x32_bf16 v[64:67], a[16:19], v[104:107], v[64:67]\n"
    "global_load_dwordx4 a[84:87], v61, s[16:17] offset:1024\n"
    "global_load_dwordx4 a[88:91], v61, s[16:17] offset:2048\n"
    "v_mfma_f32_16x16x32_bf16 v[72:75], a[12:15], v[108:111], v[72:75]\n"
    "s_waitcnt vmcnt(17)\n"
    "v_mfma_f32_16x16x32_bf16 v[68:71], v[104:107], a[20:23], v[68:71]\n"
    "global_load_dwordx4 a[92:95], v61, s[16:17] offset:3072\n"
    "v_mfma_f32_16x16x32_bf16 v[76:79], a[16:19], v[108:111], v[76:79]\n"
    "v_mfma_f32_16x16x32_bf16 v[80:83], v[108:111], a[20:23], v[80:83]\n"
    "ds_read_b128 v[104:107], v63 offset:2048\n"
    "ds_read_b128 v[108:111], v2 offset:31744\n"
    "s_waitcnt vmcnt(16)\n"
    "v_mfma_f32_16x16x32_bf16 v[64:67], a[28:31], v[112:115], v[64:67]\n"
    "v_add_u32 v61, 0x2000, v61\n"
    "global_load_dwordx4 a[96:99], v61, s[16:17] offset:-4096\n"
    "global_load_dwordx4 a[100:103], v61, s[16:17] offset:-3072\n"
    "v_mfma_f32_16x16x32_bf16 v[72:75], a[24:27], v[116:119], v[72:75]\n"
    "s_waitcnt vmcnt(17)\n"
    "v_mfma_f32_16x16x32_bf16 v[68:71], v[112:115], a[32:35], v[68:71]\n"
    "global_load_dwordx4 a[104:107], v61, s[16:17] offset:-2048\n"
    "v_mfma_f32_16x16x32_bf16 v[76:79], a[28:31], v[116:119], v[76:79]\n"
    "v_mfma_f32_16x16x32_bf16 v[80:83], v[116:119], a[32:35], v[80:83]\n"
    "ds_read_b128 v[112:115], v63 offset:2560\n"
    "ds_read_b128 v[116:119], v2 offset:32768\n"
    "s_waitcnt vmcnt(16)\n"
    "s_waitcnt lgkmcnt(2)\n"
    "v_mfma_f32_16x16x32_bf16 v[64:67], a[40:43], v[96:99], v[64:67]\n"
    "global_load_dwordx4 a[108:111], v61, s[16:17] offset:-1024\n"
    "global_load_dwordx4 a[112:115], v61, s[16:17]\n"
    "v_mfma_f32_16x16x32_bf16 v[72:75], a[36:39], v[100:103], v[72:75]\n"
    "s_waitcnt vmcnt(17)\n"
    "v_mfma_f32_16x16x32_bf16 v[68:71], v[96:99], a[44:47], v[68:71]\n"
    "global_load_dwordx4 a[116:119], v61, s[16:17] offset:1024\n"
    "v_mfma_f32_16x16x32_bf16 v[76:79], a[40:43], v[100:103], v[76:79]\n"
    "v_mfma_f32_16x16x32_bf16 v[80:83], v[100:103], a[44:47], v[80:83]\n"
    "ds_read_b128 v[96:99], v63 offset:3072\n"
    "ds_read_b128 v[100:103], v2 offset:33792\n"
    "s_waitcnt vmcnt(16)\n"
    "v_mfma_f32_16x16x32_bf16 v[64:67], a[52:55], v[104:107], v[64:67]\n"
    "global_load_dwordx4 a[120:123], v61, s[16:17] offset:2048\n"
    "global_load_dwordx4 a[124:127], v61, s[16:17] offset:3072\n"
    "v_mfma_f32_16x16x32_bf16 v[72:75], a[48:51], v[108:111], v[72:75]\n"
    "s_waitcnt vmcnt(17)\n"
    "v_mfma_f32_16x16x32_bf16 v[68:71], v[104:107], a[56:59], v[68:71]\n"
    "v_add_u32 v61, 0x2000, v61\n"
    "global_load_dwordx4 a[128:131], v61, s[16:17] offset:-4096\n"
    "v_mfma_f32_16x16x32_bf16 v[76:79], a[52:55], v[108:111], v[76:79]\n"
    "v_mfma_f32_16x16x32_bf16 v[80:83], v[108:111], a[56:59], v[80:83]\n"
    "ds_read_b128 v[104:107], v63 offset:3584\n"
    "ds_read_b128 v[108:111], v2 offset:34816\n"
    "s_waitcnt vmcnt(16)\n"
    "s_waitcnt lgkmcnt(2)\n"
    "v_mfma_f32_16x16x32_bf16 v[64:67], a[64:67], v[112:115], v[64:67]\n"
    "global_load_dwordx4 a[132:135], v61, s[16:17] offset:-3072\n"
    "global_load_dwordx4 a[136:139], v61, s[16:17] offset:-2048\n"
    "v_mfma_f32_16x16x32_bf16 v[72:75], a[60:63], v[116:119], v[72:75]\n"
    "s_waitcnt vmcnt(17)\n"
    "v_mfma_f32_16x16x32_bf16 v[68:71], v[112:115], a[68:71], v[68:71]\n"
    "global_load_dwordx4 a[140:143], v61, s[16:17] offset:-1024\n"
    "v_mfma_f32_16x16x32_bf16 v[76:79], a[64:67], v[116:119], v[76:79]\n"
    "v_mfma_f32_16x16x32_bf16 v[80:83], v[116:119], a[68:71], v[80:83]\n"
    "s_add_u32 m0, s48, 0x2400\n"
    "s_add_u32 s87, s95, 0x502000\n"
    "v_add_u32 v62, s87, v148\n"
    "global_load_lds_dwordx4 v62, s[16:17] sc1\n"
    "s_add_u32 m0, s49, 0x8c00\n"
    "s_add_u32 s87, s31, 0x502000\n"
    "v_add_u32 v62, s87, v1\n"
    "global_load_lds_dwordx4 v62, s[16:17] sc1\n"
    "global_load_lds_dwordx4 v62, s[16:17] offset:1024 sc1\n"
    "s_add_u32 m0, s48, 0x11c00\n"
    "s_add_u32 s87, s34, 0x700000\n"
    "v_add_u32 v62, s87, v1\n"
    "global_load_lds_dwordx4 v62, s[16:17] sc1\n"
    "s_add_u32 m0, s49, 0x12c00\n"
    "s_add_u32 s87, s35, 0x700000\n"
    "v_add_u32 v62, s87, v1\n"
    "global_load_lds_dwordx4 v62, s[16:17] sc1\n"
    "global_load_lds_dwordx4 v62, s[16:17] offset:1024 sc1\n"
    "global_load_dwordx4 a[144:147], v61, s[16:17]\n"
    "global_load_dwordx4 a[148:151], v61, s[16:17] offset:1024\n"
    "global_load_dwordx4 a[152:155], v61, s[16:17] offset:2048\n"
    "global_load_dwordx4 a[156:159], v61, s[16:17] offset:3072\n"
    "v_add_u32 v61, 0x2000, v61\n"
    "global_load_dwordx4 a[160:163], v61, s[16:17] offset:-4096\n"
    "global_load_dwordx4 a[164:167], v61, s[16:17] offset:-3072\n"
    "global_load_dwordx4 a[168:171], v61, s[16:17] offset:-2048\n"
    "global_load_dwordx4 a[172:175], v61, s[16:17] offset:-1024\n"
    "global_load_dwordx4 a[176:179], v61, s[16:17]\n"
    "global_load_dwordx4 a[180:183], v61, s[16:17] offset:1024\n"
    "global_load_dwordx4 a[184:187], v61, s[16:17] offset:2048\n"
    "global_load_dwordx4 a[188:191], v61, s[16:17] offset:3072\n"
    "s_mov_b32 exec_hi, 0\n"
    "global_load_dwordx4 v[156:159], v188, s[16:17] sc1\n"
    "s_mov_b64 exec, -1\n"
    "s_mov_b32 exec_hi, 0\n"
    "global_load_dwordx4 v[160:163], v188, s[16:17] offset:512 sc1\n"
    "s_mov_b64 exec, -1\n"
    "s_waitcnt vmcnt(14)\n"
    "s_barrier\n"
    "v_mfma_f32_16x16x32_bf16 v[64:67], a[76:79], v[96:99], v[64:67]\n"
    "ds_read_b128 v[112:115], v63 offset:4096\n"
    "ds_read_b128 v[116:119], v2 offset:35840\n"
    "s_mov_b32 exec_hi, 0\n"
    "global_load_dwordx4 v[164:167], v188, s[16:17] offset:1024 sc1\n"
    "s_mov_b64 exec, -1\n"
    "s_add_u32 s63, s70, 0x11c00\n"
    "v_add_u32 v147, s63, v12\n"
    "v_mfma_f32_16x16x32_bf16 v[72:75], a[72:75], v[100:103], v[72:75]\n"
    "ds_read_b64 v[128:129], v147 offset:0\n"
    "ds_read_b64 v[130:131], v147 offset:512\n"
    "v_mfma_f32_16x16x32_bf16 v[68:71], v[96:99], a[80:83], v[68:71]\n"
    "ds_read_b64 v[132:133], v147 offset:1024\n"
    "ds_read_b64 v[134:135], v147 offset:1536\n"
    "v_mfma_f32_16x16x32_bf16 v[76:79], a[76:79], v[100:103], v[76:79]\n"
    "ds_read_b64 v[136:137], v147 offset:2048\n"
    "ds_read_b64 v[138:139], v147 offset:2560\n"
    "v_mfma_f32_16x16x32_bf16 v[80:83], v[100:103], a[80:83], v[80:83]\n"
    "ds_read_b128 v[96:99], v63 offset:4608\n"
    "s_waitcnt lgkmcnt(9)\n"
    "v_mfma_f32_16x16x32_bf16 v[64:67], a[88:91], v[104:107], v[64:67]\n"
    "ds_read_b128 v[100:103], v2 offset:36864\n"
    "ds_read_b64 v[140:141], v147 offset:3072\n"
    "v_mfma_f32_16x16x32_bf16 v[72:75], a[84:87], v[108:111], v[72:75]\n"
    "ds_read_b64 v[142:143], v147 offset:3584\n"
    "v_mfma_f32_16x16x32_bf16 v[68:71], v[104:107], a[92:95], v[68:71]\n"
    "s_add_u32 m0, s50, 0xc400\n"
    "s_add_u32 s87, s96, 0x500000\n"
    "v_add_u32 v62, s87, v1\n"
    "global_load_lds_dwordx4 v62, s[16:17] sc1\n"
    "global_load_lds_dwordx4 v62, s[16:17] offset:1024 sc1\n"
    "global_load_lds_dwordx4 v62, s[16:17] offset:2048 sc1\n"
    "global_load_lds_dwordx4 v62, s[16:17] offset:3072 sc1\n"
    "v_mfma_f32_16x16x32_bf16 v[76:79], a[88:91], v[108:111], v[76:79]\n"
    "s_waitcnt lgkmcnt(4)\n"
    "v_add_f32 v128, v128, v136\n"
    "v_add_f32 v129, v129, v137\n"
    "v_mfma_f32_16x16x32_bf16 v[80:83], v[108:111], a[92:95], v[80:83]\n"
    "ds_read_b128 v[104:107], v63 offset:5120\n"
    "ds_read_b128 v[108:111], v2 offset:37888\n"
    "v_add_f32 v130, v130, v138\n"
    "v_mfma_f32_16x16x32_bf16 v[64:67], a[100:103], v[112:115], v[64:67]\n"
    "buffer_store_dwordx4 v[192:195], v208, s[16:19], 0 offen sc0 sc1\n"
    "v_add_f32 v131, v131, v139\n"
    "v_mfma_f32_16x16x32_bf16 v[72:75], a[96:99], v[116:119], v[72:75]\n"
    "s_waitcnt lgkmcnt(2)\n"
    "v_add_f32 v132, v132, v140\n"
    "v_mfma_f32_16x16x32_bf16 v[68:71], v[112:115], a[104:107], v[68:71]\n"
    "v_add_f32 v133, v133, v141\n"
    "v_add_f32 v134, v134, v142\n"
    "v_mfma_f32_16x16x32_bf16 v[76:79], a[100:103], v[116:119], v[76:79]\n"
    "buffer_store_dwordx4 v[196:199], v209, s[16:19], 0 offen sc0 sc1\n"
    "v_add_f32 v135, v135, v143\n"
    "v_mfma_f32_16x16x32_bf16 v[80:83], v[116:119], a[104:107], v[80:83]\n"
    "buffer_store_dwordx4 v[200:203], v210, s[16:19], 0 offen sc0 sc1\n"
    "buffer_store_dwordx4 v[204:207], v211, s[16:19], 0 offen sc0 sc1\n"
    "v_add_f32 v128, v128, v132\n"
    "v_mfma_f32_16x16x32_bf16 v[64:67], a[112:115], v[96:99], v[64:67]\n"
    "ds_read_b128 v[112:115], v63 offset:5632\n"
    "ds_read_b128 v[116:119], v2 offset:38912\n"
    "v_add_f32 v129, v129, v133\n"
    "v_mfma_f32_16x16x32_bf16 v[72:75], a[108:111], v[100:103], v[72:75]\n"
    "v_add_f32 v130, v130, v134\n"
    "v_add_f32 v131, v131, v135\n"
    "v_mfma_f32_16x16x32_bf16 v[68:71], v[96:99], a[116:119], v[68:71]\n"
    "v_add_f32 v128, v128, v130\n"
    "v_add_f32 v129, v129, v131\n"
    "v_mfma_f32_16x16x32_bf16 v[76:79], a[112:115], v[100:103], v[76:79]\n"
    "v_permlane32_swap_b32 v128, v129\n"
    "v_mfma_f32_16x16x32_bf16 v[80:83], v[100:103], a[116:119], v[80:83]\n"
    "ds_read_b128 v[96:99], v63 offset:6144\n"
    "s_waitcnt lgkmcnt(3)\n"
    "v_mfma_f32_16x16x32_bf16 v[64:67], a[124:127], v[104:107], v[64:67]\n"
    "ds_read_b128 v[100:103], v2 offset:39936\n"
    "v_add_f32 v128, v128, v129\n"
    "v_mfma_f32_16x16x32_bf16 v[72:75], a[120:123], v[108:111], v[72:75]\n"
    "v_mov_b32 v130, v128\n"
    "v_mfma_f32_16x16x32_bf16 v[68:71], v[104:107], a[128:131], v[68:71]\n"
    "v_permlane16_swap_b32 v128, v130\n"
    "v_mfma_f32_16x16x32_bf16 v[76:79], a[124:127], v[108:111], v[76:79]\n"
    "v_add_f32 v128, v128, v130\n"
    "v_mov_b32 v129, v128\n"
    "v_mfma_f32_16x16x32_bf16 v[80:83], v[108:111], a[128:131], v[80:83]\n"
    "ds_read_b128 v[104:107], v63 offset:6656\n"
    "s_waitcnt lgkmcnt(3)\n"
    "v_mfma_f32_16x16x32_bf16 v[64:67], a[136:139], v[112:115], v[64:67]\n"
    "ds_read_b128 v[108:111], v2 offset:40960\n"
    "v_permlane32_swap_b32 v128, v129\n"
    "v_mfma_f32_16x16x32_bf16 v[72:75], a[132:135], v[116:119], v[72:75]\n"
    "v_mul_f32 v131, s79, v128\n"
    "v_mov_b32 v132, s81\n"
    "v_mfma_f32_16x16x32_bf16 v[68:71], v[112:115], a[140:143], v[68:71]\n"
    "v_fma_f32 v132, v129, s80, v132\n"
    "v_fma_f32 v132, -v131, v131, v132\n"
    "v_mfma_f32_16x16x32_bf16 v[76:79], a[136:139], v[116:119], v[76:79]\n"
    "v_max_f32 v132, s81, v132\n"
    "v_mfma_f32_16x16x32_bf16 v[80:83], v[116:119], a[140:143], v[80:83]\n"
    "ds_read_b128 v[112:115], v63 offset:7168\n"
    "s_waitcnt vmcnt(14)\n"
    "s_waitcnt lgkmcnt(3)\n"
    "v_mfma_f32_16x16x32_bf16 v[64:67], a[148:151], v[96:99], v[64:67]\n"
    "ds_read_b128 v[116:119], v2 offset:41984\n"
    "v_mfma_f32_16x16x32_bf16 v[72:75], a[144:147], v[100:103], v[72:75]\n"
    "v_rsq_f32 v133, v132\n"
    "v_mfma_f32_16x16x32_bf16 v[68:71], v[96:99], a[152:155], v[68:71]\n"
    "v_cvt_pk_bf16_f32 v136, v131, v131\n"
    "v_lshlrev_b32 v134, 16, v136\n"
    "v_mfma_f32_16x16x32_bf16 v[76:79], a[148:151], v[100:103], v[76:79]\n"
    "v_sub_f32 v134, v131, v134\n"
    "v_cvt_pk_bf16_f32 v137, v134, 0\n"
    "v_mfma_f32_16x16x32_bf16 v[80:83], v[100:103], a[152:155], v[80:83]\n"
    "ds_read_b128 v[96:99], v63 offset:7680\n"
    "s_waitcnt lgkmcnt(3)\n"
    "v_mfma_f32_16x16x32_bf16 v[64:67], a[160:163], v[104:107], v[64:67]\n"
    "ds_read_b128 v[100:103], v2 offset:43008\n"
    "v_add_u32 v147, s72, v150\n"
    "v_mfma_f32_16x16x32_bf16 v[72:75], a[156:159], v[108:111], v[72:75]\n"
    "ds_write_b64 v147, v[136:137] offset:0\n"
    "v_mfma_f32_16x16x32_bf16 v[68:71], v[104:107], a[164:167], v[68:71]\n"
    "v_add_u32 v147, s74, v14\n"
    "v_mfma_f32_16x16x32_bf16 v[76:79], a[160:163], v[108:111], v[76:79]\n"
    "ds_write_b32 v147, v133 offset:1536\n"
    "v_mfma_f32_16x16x32_bf16 v[80:83], v[108:111], a[164:167], v[80:83]\n"
    "s_waitcnt vmcnt(10)\n"
    "v_add_f32 v156, v156, v157\n"
    "v_add_f32 v158, v158, v159\n"
    "v_add_f32 v156, v156, v158\n"
    "v_cvt_pk_bf16_f32 v49, v156, 0\n"
    "v_lshlrev_b32 v189, 16, v49\n"
    "v_sub_f32 v189, v156, v189\n"
    "v_cvt_pk_bf16_f32 v48, v156, v189\n"
    "s_waitcnt vmcnt(9)\n"
    "v_add_f32 v160, v160, v161\n"
    "v_add_f32 v162, v162, v163\n"
    "v_add_f32 v160, v160, v162\n"
    "v_cvt_pk_bf16_f32 v51, v160, 0\n"
    "v_lshlrev_b32 v189, 16, v51\n"
    "v_sub_f32 v189, v160, v189\n"
    "v_cvt_pk_bf16_f32 v50, v160, v189\n"
    "s_waitcnt vmcnt(8)\n"
    "v_add_f32 v164, v164, v165\n"
    "v_add_f32 v166, v166, v167\n"
    "v_add_f32 v164, v164, v166\n"
    "v_cvt_pk_bf16_f32 v53, v164, 0\n"
    "v_lshlrev_b32 v189, 16, v53\n"
    "v_sub_f32 v189, v164, v189\n"
    "v_cvt_pk_bf16_f32 v52, v164, v189\n"
    "s_waitcnt vmcnt(3)\n"
    "s_waitcnt lgkmcnt(0)\n"
    "s_barrier\n"
    "v_mfma_f32_16x16x32_bf16 v[64:67], a[172:175], v[112:115], v[64:67]\n"
    "ds_read_b64 v[124:125], v15 offset:1024\n"
    "ds_read_b64 v[120:121], v15 offset:0\n"
    "s_add_u32 m0, s48, 0x16000\n"
    "s_add_u32 s87, s95, 0x508000\n"
    "v_add_u32 v62, s87, v148\n"
    "global_load_lds_dwordx4 v62, s[16:17] sc1\n"
    "s_add_u32 m0, s49, 0x1a000\n"
    "s_add_u32 s87, s31, 0x508000\n"
    "v_add_u32 v62, s87, v1\n"
    "global_load_lds_dwordx4 v62, s[16:17] sc1\n"
    "global_load_lds_dwordx4 v62, s[16:17] offset:1024 sc1\n"
    "v_mfma_f32_16x16x32_bf16 v[72:75], a[168:171], v[116:119], v[72:75]\n"
    "ds_read_b64 v[122:123], v15 offset:512\n"
    "ds_read_b128 v[104:107], v3 offset:1024\n"
    "v_mfma_f32_16x16x32_bf16 v[68:71], v[112:115], a[176:179], v[68:71]\n"
    "ds_read_b128 v[108:111], v3 offset:2048\n"
    "ds_read_b32 v144, v14 offset:1536\n"
    "s_add_u32 m0, s48, 0x17000\n"
    "s_add_u32 s87, s95, 0x50a000\n"
    "v_add_u32 v62, s87, v148\n"
    "global_load_lds_dwordx4 v62, s[16:17] sc1\n"
    "v_mfma_f32_16x16x32_bf16 v[76:79], a[172:175], v[116:119], v[76:79]\n"
    "ds_read_b32 v145, v14 offset:1600\n"
    "ds_read_b32 v146, v14 offset:1664\n"
    "v_mfma_f32_16x16x32_bf16 v[80:83], v[116:119], a[176:179], v[80:83]\n"
    "ds_read_b128 v[112:115], v13 offset:1536\n"
    "ds_read_b128 v[116:119], v13 offset:1600\n"
    "v_mfma_f32_16x16x32_bf16 v[64:67], a[184:187], v[96:99], v[64:67]\n"
    "v_mfma_f32_16x16x32_bf16 v[72:75], a[180:183], v[100:103], v[72:75]\n"
    "s_add_u32 m0, s49, 0x1c000\n"
    "s_add_u32 s87, s31, 0x50a000\n"
    "v_add_u32 v62, s87, v1\n"
    "global_load_lds_dwordx4 v62, s[16:17] sc1\n"
    "global_load_lds_dwordx4 v62, s[16:17] offset:1024 sc1\n"
    "v_mfma_f32_16x16x32_bf16 v[68:71], v[96:99], a[188:191], v[68:71]\n"
    "v_mfma_f32_16x16x32_bf16 v[76:79], a[184:187], v[100:103], v[76:79]\n"
    "v_mfma_f32_16x16x32_bf16 v[80:83], v[100:103], a[188:191], v[80:83]\n"
    "ds_read_b128 v[96:99], v3 offset:3072\n"
    "s_waitcnt vmcnt(6)\n"
    "s_barrier\n"
    "v_mov_b32 v62, s98\n"
    "s_mov_b32 s83, %[offB]\n"
    "s_mov_b64 exec, s[68:69]\n"
    "buffer_store_dword v62, off, s[16:19], s83 sc0 sc1\n"
    "s_mov_b64 exec, -1\n"
    "s_waitcnt lgkmcnt(1)\n"
    "v_mfma_f32_16x16x16_bf16 v[84:87], v[48:49], v[124:125], 0\n"
    "ds_read_b128 v[100:103], v3 offset:4096\n"
    "v_mfma_f32_16x16x16_bf16 v[88:91], v[50:51], v[124:125], 0\n"
    "v_mfma_f32_16x16x16_bf16 v[92:95], v[124:125], v[52:53], 0\n"
    "v_mfma_f32_16x16x16_bf16 v[64:67], v[50:51], v[120:121], v[64:67]\n"
    "v_mfma_f32_16x16x16_bf16 v[72:75], v[48:49], v[122:123], v[72:75]\n"
    "v_mfma_f32_16x16x16_bf16 v[76:79], v[50:51], v[122:123], v[76:79]\n"
    "v_mfma_f32_16x16x16_bf16 v[68:71], v[120:121], v[52:53], v[68:71]\n"
    "s_add_u32 m0, s48, 0x22000\n"
    "s_add_u32 s87, s34, 0x702000\n"
    "v_add_u32 v62, s87, v1\n"
    "global_load_lds_dwordx4 v62, s[16:17] sc1\n"
    "v_mfma_f32_16x16x16_bf16 v[80:83], v[122:123], v[52:53], v[80:83]\n"
    "v_mul_f32 v64, v64, v144\n"
    "v_mul_f32 v65, v65, v144\n"
    "v_mfma_f32_16x16x32_bf16 v[84:87], a[0:3], v[104:107], v[84:87]\n"
    "v_mul_f32 v66, v66, v144\n"
    "v_mul_f32 v67, v67, v144\n"
    "v_mfma_f32_16x16x32_bf16 v[88:91], a[4:7], v[104:107], v[88:91]\n"
    "s_add_u32 s88, s42, 0x500000\n"
    "buffer_load_dwordx4 v[180:183], v8, s[16:19], s88 offen sc1\n"
    "s_add_u32 s88, s42, 0x508000\n"
    "buffer_load_dwordx4 v[184:187], v8, s[16:19], s88 offen sc1\n"
    "v_mfma_f32_16x16x32_bf16 v[92:95], v[104:107], a[8:11], v[92:95]\n"
    "ds_read_b128 v[104:107], v3 offset:5120\n"
    "v_cvt_pk_bf16_f32 v126, v64, v65\n"
    "v_cvt_pk_bf16_f32 v127, v66, v67\n"
    "v_mfma_f32_16x16x32_bf16 v[84:87], a[12:15], v[108:111], v[84:87]\n"
    "v_cvt_pk_bf16_f32 v132, v72, v73\n"
    "v_cvt_pk_bf16_f32 v133, v74, v75\n"
    "v_mfma_f32_16x16x32_bf16 v[88:91], a[16:19], v[108:111], v[88:91]\n"
    "v_mul_f32 v76, v76, v145\n"
    "v_mul_f32 v77, v77, v145\n"
    "v_mfma_f32_16x16x32_bf16 v[92:95], v[108:111], a[20:23], v[92:95]\n"
    "ds_read_b128 v[108:111], v3 offset:6144\n"
    "v_mul_f32 v78, v78, v145\n"
    "s_waitcnt lgkmcnt(2)\n"
    "v_mfma_f32_16x16x32_bf16 v[84:87], a[24:27], v[96:99], v[84:87]\n"
    "v_mul_f32 v79, v79, v145\n"
    "v_cvt_pk_bf16_f32 v128, v76, v77\n"
    "v_mfma_f32_16x16x32_bf16 v[88:91], a[28:31], v[96:99], v[88:91]\n"
    "v_cvt_pk_bf16_f32 v129, v78, v79\n"
    "v_mul_f32 v68, v68, v112\n"
    "s_add_u32 m0, s49, 0x23000\n"
    "s_add_u32 s87, s35, 0x702000\n"
    "v_add_u32 v62, s87, v1\n"
    "global_load_lds_dwordx4 v62, s[16:17] sc1\n"
    "global_load_lds_dwordx4 v62, s[16:17] offset:1024 sc1\n"
    "v_mfma_f32_16x16x32_bf16 v[92:95], v[96:99], a[32:35], v[92:95]\n"
    "ds_read_b128 v[96:99], v3 offset:7168\n"
    "v_mul_f32 v69, v69, v113\n"
    "v_mfma_f32_16x16x32_bf16 v[84:87], a[36:39], v[100:103], v[84:87]\n"
    "v_mul_f32 v70, v70, v114\n"
    "v_mul_f32 v71, v71, v115\n"
    "v_mfma_f32_16x16x32_bf16 v[88:91], a[40:43], v[100:103], v[88:91]\n"
    "v_cvt_pk_bf16_f32 v120, v68, v69\n"
    "v_cvt_pk_bf16_f32 v121, v70, v71\n"
    "v_mfma_f32_16x16x32_bf16 v[92:95], v[100:103], a[44:47], v[92:95]\n"
    "ds_read_b128 v[100:103], v3 offset:8192\n"
    "ds_read_b128 v[112:115], v13 offset:1664\n"
    "v_mul_f32 v80, v80, v116\n"
    "s_waitcnt lgkmcnt(3)\n"
    "v_mfma_f32_16x16x32_bf16 v[84:87], a[48:51], v[104:107], v[84:87]\n"
    "v_mul_f32 v81, v81, v117\n"
    "v_mul_f32 v82, v82, v118\n"
    "v_mfma_f32_16x16x32_bf16 v[88:91], a[52:55], v[104:107], v[88:91]\n"
    "s_add_u32 m0, s50, 0x1e000\n"
    "s_add_u32 s87, s96, 0x508000\n"
    "v_add_u32 v62, s87, v1\n"
    "global_load_lds_dwordx4 v62, s[16:17] sc1\n"
    "global_load_lds_dwordx4 v62, s[16:17] offset:1024 sc1\n"
    "global_load_lds_dwordx4 v62, s[16:17] offset:2048 sc1\n"
    "global_load_lds_dwordx4 v62, s[16:17] offset:3072 sc1\n"
    "v_mul_f32 v83, v83, v119\n"
    "v_mfma_f32_16x16x32_bf16 v[92:95], v[104:107], a[56:59], v[92:95]\n"
    "ds_read_b128 v[104:107], v3 offset:9216\n"
    "v_cvt_pk_bf16_f32 v122, v80, v81\n"
    "v_cvt_pk_bf16_f32 v123, v82, v83\n"
    "v_mfma_f32_16x16x32_bf16 v[84:87], a[60:63], v[108:111], v[84:87]\n"
    "v_mfma_f32_16x16x16_bf16 v[64:67], v[126:127], v[132:133], v[32:35]\n"
    "v_mfma_f32_16x16x16_bf16 v[68:71], v[128:129], v[132:133], v[40:43]\n"
    "v_mul_f32 v142, s76, v145\n"
    "v_mfma_f32_16x16x32_bf16 v[88:91], a[64:67], v[108:111], v[88:91]\n"
    "v_max3_f32 v140, v64, v65, v66\n"
    "v_mfma_f32_16x16x32_bf16 v[92:95], v[108:111], a[68:71], v[92:95]\n"
    "ds_read_b128 v[108:111], v3 offset:10240\n"
    "s_waitcnt lgkmcnt(2)\n"
    "v_mfma_f32_16x16x32_bf16 v[84:87], a[72:75], v[96:99], v[84:87]\n"
    "v_max3_f32 v141, v67, v68, v69\n"
    "v_max3_f32 v140, v140, v70, v71\n"
    "v_mfma_f32_16x16x32_bf16 v[88:91], a[76:79], v[96:99], v[88:91]\n"
    "v_max_f32 v140, v140, v141\n"
    "v_mov_b32 v141, v140\n"
    "v_mfma_f32_16x16x32_bf16 v[92:95], v[96:99], a[80:83], v[92:95]\n"
    "ds_read_b128 v[96:99], v3 offset:11264\n"
    "v_permlane16_swap_b32 v141, v140\n"
    "v_mfma_f32_16x16x32_bf16 v[84:87], a[84:87], v[100:103], v[84:87]\n"
    "v_max_f32 v140, v140, v141\n"
    "v_mov_b32 v141, v140\n"
    "v_mfma_f32_16x16x32_bf16 v[88:91], a[88:91], v[100:103], v[88:91]\n"
    "v_permlane32_swap_b32 v141, v140\n"
    "v_mfma_f32_16x16x32_bf16 v[92:95], v[100:103], a[92:95], v[92:95]\n"
    "ds_read_b128 v[100:103], v3 offset:12288\n"
    "s_waitcnt lgkmcnt(3)\n"
    "v_mfma_f32_16x16x32_bf16 v[84:87], a[96:99], v[104:107], v[84:87]\n"
    "v_max_f32 v140, v140, v141\n"
    "v_mul_f32 v140, v142, v140\n"
    "v_mfma_f32_16x16x32_bf16 v[88:91], a[100:103], v[104:107], v[88:91]\n"
    "v_fma_f32 v64, v64, v142, -v140\n"
    "v_fma_f32 v65, v65, v142, -v140\n"
    "v_mfma_f32_16x16x32_bf16 v[92:95], v[104:107], a[104:107], v[92:95]\n"
    "ds_read_b128 v[104:107], v3 offset:13312\n"
    "v_fma_f32 v66, v66, v142, -v140\n"
    "s_waitcnt lgkmcnt(3)\n"
    "v_mfma_f32_16x16x32_bf16 v[84:87], a[108:111], v[108:111], v[84:87]\n"
    "v_fma_f32 v67, v67, v142, -v140\n"
    "v_fma_f32 v68, v68, v142, -v140\n"
    "v_mfma_f32_16x16x32_bf16 v[88:91], a[112:115], v[108:111], v[88:91]\n"
    "v_fma_f32 v69, v69, v142, -v140\n"
    "v_fma_f32 v70, v70, v142, -v140\n"
    "v_mfma_f32_16x16x32_bf16 v[92:95], v[108:111], a[116:119], v[92:95]\n"
    "ds_read_b128 v[108:111], v3 offset:14336\n"
    "v_fma_f32 v71, v71, v142, -v140\n"
    "v_exp_f32 v64, v64\n"
    "s_waitcnt lgkmcnt(2)\n"
    "v_mfma_f32_16x16x32_bf16 v[84:87], a[120:123], v[96:99], v[84:87]\n"
    "v_exp_f32 v65, v65\n"
    "v_mfma_f32_16x16x32_bf16 v[88:91], a[124:127], v[96:99], v[88:91]\n"
    "v_exp_f32 v66, v66\n"
    "v_mfma_f32_16x16x32_bf16 v[92:95], v[96:99], a[128:131], v[92:95]\n"
    "ds_read_b128 v[96:99], v3 offset:15360\n"
    "v_mfma_f32_16x16x32_bf16 v[84:87], a[132:135], v[100:103], v[84:87]\n"
    "v_exp_f32 v67, v67\n"
    "v_mfma_f32_16x16x32_bf16 v[88:91], a[136:139], v[100:103], v[88:91]\n"
    "v_exp_f32 v68, v68\n"
    "v_mfma_f32_16x16x32_bf16 v[92:95], v[100:103], a[140:143], v[92:95]\n"
    "ds_read_b128 v[100:103], v3 offset:16384\n"
    "v_exp_f32 v69, v69\n"
    "s_waitcnt lgkmcnt(2)\n"
    "v_mfma_f32_16x16x32_bf16 v[84:87], a[144:147], v[104:107], v[84:87]\n"
    "v_exp_f32 v70, v70\n"
    "v_mfma_f32_16x16x32_bf16 v[88:91], a[148:151], v[104:107], v[88:91]\n"
    "v_exp_f32 v71, v71\n"
    "v_mfma_f32_16x16x32_bf16 v[92:95], v[104:107], a[152:155], v[92:95]\n"
    "v_cvt_pk_bf16_f32 v72, v64, v65\n"
    "v_cvt_pk_bf16_f32 v73, v66, v67\n"
    "v_mfma_f32_16x16x32_bf16 v[84:87], a[156:159], v[108:111], v[84:87]\n"
    "v_cvt_pk_bf16_f32 v74, v68, v69\n"
    "v_cvt_pk_bf16_f32 v75, v70, v71\n"
    "v_mfma_f32_16x16x32_bf16 v[88:91], a[160:163], v[108:111], v[88:91]\n"
    "v_mfma_f32_16x16x32_bf16 v[80:83], v[44:47], v[72:75], 0\n"
    "v_mfma_f32_16x16x32_bf16 v[76:79], v[120:123], v[72:75], 0\n"
    "v_mfma_f32_16x16x32_bf16 v[92:95], v[108:111], a[164:167], v[92:95]\n"
    "s_waitcnt lgkmcnt(0)\n"
    "v_mfma_f32_16x16x32_bf16 v[84:87], a[168:171], v[96:99], v[84:87]\n"
    "v_rcp_f32 v141, v80\n"
    "v_mfma_f32_16x16x32_bf16 v[88:91], a[172:175], v[96:99], v[88:91]\n"
    "v_mul_f32 v76, v76, v141\n"
    "v_mul_f32 v77, v77, v141\n"
    "v_mfma_f32_16x16x32_bf16 v[92:95], v[96:99], a[176:179], v[92:95]\n"
    "v_mul_f32 v78, v78, v141\n"
    "v_mul_f32 v79, v79, v141\n"
    "v_mfma_f32_16x16x32_bf16 v[84:87], a[180:183], v[100:103], v[84:87]\n"
    "v_cvt_pk_bf16_f32 v136, v76, v77\n"
    "v_cvt_pk_bf16_f32 v137, v78, v79\n"
    "v_mfma_f32_16x16x32_bf16 v[88:91], a[184:187], v[100:103], v[88:91]\n"
    "v_mfma_f32_16x16x32_bf16 v[92:95], v[100:103], a[188:191], v[92:95]\n"
    "s_nop 3\n"
    "v_mul_f32 v88, v88, v146\n"
    "v_mul_f32 v89, v89, v146\n"
    "v_mul_f32 v90, v90, v146\n"
    "v_mul_f32 v91, v91, v146\n"
    "v_cvt_pk_bf16_f32 v130, v88, v89\n"
    "v_cvt_pk_bf16_f32 v131, v90, v91\n"
    "v_cvt_pk_bf16_f32 v134, v84, v85\n"
    "v_cvt_pk_bf16_f32 v135, v86, v87\n"
    "s_nop 1\n"
    "v_mfma_f32_16x16x16_bf16 v[64:67], v[128:129], v[134:135], v[36:39]\n"
    "v_mfma_f32_16x16x16_bf16 v[68:71], v[130:131], v[134:135], v[40:43]\n"
    "s_waitcnt vmcnt(7)\n"
    "v_lshlrev_b32 v16, 16, v180\n"
    "v_and_b32 v17, 0xffff0000, v180\n"
    "v_lshlrev_b32 v18, 16, v181\n"
    "v_and_b32 v19, 0xffff0000, v181\n"
    "v_lshlrev_b32 v20, 16, v182\n"
    "v_and_b32 v21, 0xffff0000, v182\n"
    "v_lshlrev_b32 v22, 16, v183\n"
    "v_and_b32 v23, 0xffff0000, v183\n"
    "v_mul_f32 v142, s76, v146\n"
    "v_mul_f32 v92, v92, v112\n"
    "v_mul_f32 v93, v93, v113\n"
    "v_mul_f32 v94, v94, v114\n"
    "v_mul_f32 v95, v95, v115\n"
    "v_cvt_pk_bf16_f32 v124, v92, v93\n"
    "v_cvt_pk_bf16_f32 v125, v94, v95\n"
    "s_waitcnt vmcnt(6)\n"
    "v_lshlrev_b32 v24, 16, v184\n"
    "v_and_b32 v25, 0xffff0000, v184\n"
    "v_lshlrev_b32 v26, 16, v185\n"
    "v_and_b32 v27, 0xffff0000, v185\n"
    "v_lshlrev_b32 v28, 16, v186\n"
    "v_and_b32 v29, 0xffff0000, v186\n"
    "v_lshlrev_b32 v30, 16, v187\n"
    "v_and_b32 v31, 0xffff0000, v187\n"
    "v_max3_f32 v140, v64, v65, v66\n"
    "v_max3_f32 v141, v67, v68, v69\n"
    "v_max3_f32 v140, v140, v70, v71\n"
    "v_max_f32 v140, v140, v141\n"
    "v_mov_b32 v141, v140\n"
    "s_nop 1\n"
    "v_permlane16_swap_b32 v141, v140\n"
    "v_max_f32 v140, v140, v141\n"
    "v_mov_b32 v141, v140\n"
    "s_nop 1\n"
    "v_permlane32_swap_b32 v141, v140\n"
    "v_max_f32 v140, v140, v141\n"
    "v_mul_f32 v140, v142, v140\n"
    "v_fma_f32 v64, v64, v142, -v140\n"
    "v_fma_f32 v65, v65, v142, -v140\n"
    "v_fma_f32 v66, v66, v142, -v140\n"
    "v_fma_f32 v67, v67, v142, -v140\n"
    "v_fma_f32 v68, v68, v142, -v140\n"
    "v_fma_f32 v69, v69, v142, -v140\n"
    "v_fma_f32 v70, v70, v142, -v140\n"
    "v_fma_f32 v71, v71, v142, -v140\n"
    "v_exp_f32 v64, v64\n"
    "v_exp_f32 v65, v65\n"
    "v_exp_f32 v66, v66\n"
    "v_exp_f32 v67, v67\n"
    "v_exp_f32 v68, v68\n"
    "v_exp_f32 v69, v69\n"
    "v_exp_f32 v70, v70\n"
    "v_exp_f32 v71, v71\n"
    "v_cvt_pk_bf16_f32 v72, v64, v65\n"
    "v_cvt_pk_bf16_f32 v73, v66, v67\n"
    "v_cvt_pk_bf16_f32 v74, v68, v69\n"
    "v_cvt_pk_bf16_f32 v75, v70, v71\n"
    "s_nop 1\n"
    "v_mfma_f32_16x16x32_bf16 v[80:83], v[44:47], v[72:75], 0\n"
    "v_mfma_f32_16x16x32_bf16 v[76:79], v[122:125], v[72:75], 0\n"
    "s_nop 3\n"
    "v_rcp_f32 v141, v80\n"
    "s_nop 2\n"
    "v_mul_f32 v76, v76, v141\n"
    "v_mul_f32 v77, v77, v141\n"
    "v_mul_f32 v78, v78, v141\n"
    "v_mul_f32 v79, v79, v141\n"
    "v_cvt_pk_bf16_f32 v138, v76, v77\n"
    "v_cvt_pk_bf16_f32 v139, v78, v79\n"
    "s_nop 0\n"
    "v_permlane16_swap_b32 v136, v138\n"
    "v_permlane16_swap_b32 v137, v139\n"
    "s_add_u32 s84, s44, 0x780000\n"
    "buffer_store_dwordx4 v[136:139], v1, s[16:19], s84 offen sc1\n"
    "s_barrier\n"
    "v_add_u32 v63, 0x16000, v149\n"
    "ds_read_b128 v[96:99], v63 offset:0\n"
    "ds_read_b128 v[100:103], v4 offset:8192\n"
    "ds_read_b128 v[104:107], v63 offset:512\n"
    "ds_read_b128 v[108:111], v4 offset:9216\n"
    "ds_read_b128 v[112:115], v63 offset:1024\n"
    "ds_read_b128 v[116:119], v4 offset:10240\n"
    "s_waitcnt lgkmcnt(4)\n"
    "v_mfma_f32_16x16x32_bf16 v[64:67], a[4:7], v[96:99], 0\n"
    "v_mfma_f32_16x16x32_bf16 v[72:75], a[0:3], v[100:103], 0\n"
    "v_mfma_f32_16x16x32_bf16 v[68:71], v[96:99], a[8:11], 0\n"
    "v_mfma_f32_16x16x32_bf16 v[76:79], a[4:7], v[100:103], 0\n"
    "v_mfma_f32_16x16x32_bf16 v[80:83], v[100:103], a[8:11], 0\n"
    "ds_read_b128 v[96:99], v63 offset:1536\n"
    "s_waitcnt lgkmcnt(1)\n"
    "v_mfma_f32_16x16x32_bf16 v[64:67], a[16:19], v[104:107], v[64:67]\n"
    "ds_read_b128 v[100:103], v4 offset:11264\n"
    "v_mfma_f32_16x16x32_bf16 v[72:75], a[12:15], v[108:111], v[72:75]\n"
    "v_mfma_f32_16x16x32_bf16 v[68:71], v[104:107], a[20:23], v[68:71]\n"
    "v_mfma_f32_16x16x32_bf16 v[76:79], a[16:19], v[108:111], v[76:79]\n"
    "s_mov_b64 exec, 0x7fffffff\n"
    "buffer_load_dword v152, v153, s[16:19], 0 offen sc1\n"
    "s_mov_b64 exec, -1\n"
    "v_mfma_f32_16x16x32_bf16 v[80:83], v[108:111], a[20:23], v[80:83]\n"
    "ds_read_b128 v[104:107], v63 offset:2048\n"
    "ds_read_b128 v[108:111], v4 offset:12288\n"
    "v_mfma_f32_16x16x32_bf16 v[64:67], a[28:31], v[112:115], v[64:67]\n"
    "v_mfma_f32_16x16x32_bf16 v[72:75], a[24:27], v[116:119], v[72:75]\n"
    "v_mfma_f32_16x16x32_bf16 v[68:71], v[112:115], a[32:35], v[68:71]\n"
    "v_mfma_f32_16x16x32_bf16 v[76:79], a[28:31], v[116:119], v[76:79]\n"
    "v_mfma_f32_16x16x32_bf16 v[80:83], v[116:119], a[32:35], v[80:83]\n"
    "ds_read_b128 v[112:115], v63 offset:2560\n"
    "s_waitcnt lgkmcnt(3)\n"
    "v_mfma_f32_16x16x32_bf16 v[64:67], a[40:43], v[96:99], v[64:67]\n"
    "ds_read_b128 v[116:119], v4 offset:13312\n"
    "v_mfma_f32_16x16x32_bf16 v[72:75], a[36:39], v[100:103], v[72:75]\n"
    "v_mfma_f32_16x16x32_bf16 v[68:71], v[96:99], a[44:47], v[68:71]\n"
    "v_mfma_f32_16x16x32_bf16 v[76:79], a[40:43], v[100:103], v[76:79]\n"
    "v_mfma_f32_16x16x32_bf16 v[80:83], v[100:103], a[44:47], v[80:83]\n"
    "ds_read_b128 v[96:99], v63 offset:3072\n"
    "s_waitcnt lgkmcnt(3)\n"
    "v_mfma_f32_16x16x32_bf16 v[64:67], a[52:55], v[104:107], v[64:67]\n"
    "ds_read_b128 v[100:103], v4 offset:14336\n"
    "v_mfma_f32_16x16x32_bf16 v[72:75], a[48:51], v[108:111], v[72:75]\n"
    "v_mfma_f32_16x16x32_bf16 v[68:71], v[104:107], a[56:59], v[68:71]\n"
    "v_mfma_f32_16x16x32_bf16 v[76:79], a[52:55], v[108:111], v[76:79]\n"
    "v_mfma_f32_16x16x32_bf16 v[80:83], v[108:111], a[56:59], v[80:83]\n"
    "ds_read_b128 v[104:107], v63 offset:3584\n"
    "s_waitcnt lgkmcnt(3)\n"
    "v_mfma_f32_16x16x32_bf16 v[64:67], a[64:67], v[112:115], v[64:67]\n"
    "ds_read_b128 v[108:111], v4 offset:15360\n"
    "v_mfma_f32_16x16x32_bf16 v[72:75], a[60:63], v[116:119], v[72:75]\n"
    "v_mfma_f32_16x16x32_bf16 v[68:71], v[112:115], a[68:71], v[68:71]\n"
    "v_mfma_f32_16x16x32_bf16 v[76:79], a[64:67], v[116:119], v[76:79]\n"
    "v_mfma_f32_16x16x32_bf16 v[80:83], v[116:119], a[68:71], v[80:83]\n"
    "s_waitcnt vmcnt(2)\n"
    "s_barrier\n"
    "s_waitcnt lgkmcnt(2)\n"
    "v_mfma_f32_16x16x32_bf16 v[64:67], a[76:79], v[96:99], v[64:67]\n"
    "ds_read_b128 v[112:115], v63 offset:4096\n"
    "ds_read_b128 v[116:119], v4 offset:16384\n"
    "s_add_u32 s63, s70, 0x22000\n"
    "v_add_u32 v147, s63, v12\n"
    "v_mfma_f32_16x16x32_bf16 v[72:75], a[72:75], v[100:103], v[72:75]\n"
    "ds_read_b64 v[128:129], v147 offset:0\n"
    "ds_read_b64 v[130:131], v147 offset:512\n"
    "v_mfma_f32_16x16x32_bf16 v[68:71], v[96:99], a[80:83], v[68:71]\n"
    "ds_read_b64 v[132:133], v147 offset:1024\n"
    "ds_read_b64 v[134:135], v147 offset:1536\n"
    "v_mfma_f32_16x16x32_bf16 v[76:79], a[76:79], v[100:103], v[76:79]\n"
    "ds_read_b64 v[136:137], v147 offset:2048\n"
    "ds_read_b64 v[138:139], v147 offset:2560\n"
    "v_mfma_f32_16x16x32_bf16 v[80:83], v[100:103], a[80:83], v[80:83]\n"
    "ds_read_b128 v[96:99], v63 offset:4608\n"
    "s_waitcnt lgkmcnt(9)\n"
    "v_mfma_f32_16x16x32_bf16 v[64:67], a[88:91], v[104:107], v[64:67]\n"
    "ds_read_b128 v[100:103], v4 offset:17408\n"
    "ds_read_b64 v[140:141], v147 offset:3072\n"
    "v_mfma_f32_16x16x32_bf16 v[72:75], a[84:87], v[108:111], v[72:75]\n"
    "ds_read_b64 v[142:143], v147 offset:3584\n"
    "v_mfma_f32_16x16x32_bf16 v[68:71], v[104:107], a[92:95], v[68:71]\n"
    "v_mfma_f32_16x16x32_bf16 v[76:79], a[88:91], v[108:111], v[76:79]\n"
    "s_waitcnt vmcnt(1)\n"
    "s_barrier\n"
    "v_mov_b32 v62, s98\n"
    "s_add_u32 s83, s47, 0x1f10000\n"
    "s_mov_b64 exec, s[68:69]\n"
    "buffer_store_dword v62, off, s[16:19], s83 sc1\n"
    "s_mov_b64 exec, -1\n"
    "s_waitcnt lgkmcnt(4)\n"
    "v_add_f32 v128, v128, v136\n"
    "v_add_f32 v129, v129, v137\n"
    "v_mfma_f32_16x16x32_bf16 v[80:83], v[108:111], a[92:95], v[80:83]\n"
    "ds_read_b128 v[104:107], v63 offset:5120\n"
    "ds_read_b128 v[108:111], v4 offset:18432\n"
    "v_add_f32 v130, v130, v138\n"
    "v_mfma_f32_16x16x32_bf16 v[64:67], a[100:103], v[112:115], v[64:67]\n"
    "v_add_f32 v131, v131, v139\n"
    "s_waitcnt lgkmcnt(2)\n"
    "v_add_f32 v132, v132, v140\n"
    "v_mfma_f32_16x16x32_bf16 v[72:75], a[96:99], v[116:119], v[72:75]\n"
    "v_add_f32 v133, v133, v141\n"
    "v_add_f32 v134, v134, v142\n"
    "v_mfma_f32_16x16x32_bf16 v[68:71], v[112:115], a[104:107], v[68:71]\n"
    "v_add_f32 v135, v135, v143\n"
    "v_add_f32 v128, v128, v132\n"
    "v_mfma_f32_16x16x32_bf16 v[76:79], a[100:103], v[116:119], v[76:79]\n"
    "v_add_f32 v129, v129, v133\n"
    "v_add_f32 v130, v130, v134\n"
    "v_mfma_f32_16x16x32_bf16 v[80:83], v[116:119], a[104:107], v[80:83]\n"
    "ds_read_b128 v[112:115], v63 offset:5632\n"
    "ds_read_b128 v[116:119], v4 offset:19456\n"
    "v_mfma_f32_16x16x32_bf16 v[64:67], a[112:115], v[96:99], v[64:67]\n"
    "v_add_f32 v131, v131, v135\n"
    "v_add_f32 v128, v128, v130\n"
    "v_mfma_f32_16x16x32_bf16 v[72:75], a[108:111], v[100:103], v[72:75]\n"
    "v_add_f32 v129, v129, v131\n"
    "v_mfma_f32_16x16x32_bf16 v[68:71], v[96:99], a[116:119], v[68:71]\n"
    "v_permlane32_swap_b32 v128, v129\n"
    "v_mfma_f32_16x16x32_bf16 v[76:79], a[112:115], v[100:103], v[76:79]\n"
    "v_add_f32 v128, v128, v129\n"
    "v_mov_b32 v130, v128\n"
    "v_mfma_f32_16x16x32_bf16 v[80:83], v[100:103], a[116:119], v[80:83]\n"
    "ds_read_b128 v[96:99], v63 offset:6144\n"
    "s_waitcnt lgkmcnt(3)\n"
    "v_mfma_f32_16x16x32_bf16 v[64:67], a[124:127], v[104:107], v[64:67]\n"
    "ds_read_b128 v[100:103], v4 offset:20480\n"
    "v_mfma_f32_16x16x32_bf16 v[72:75], a[120:123], v[108:111], v[72:75]\n"
    "v_permlane16_swap_b32 v128, v130\n"
    "v_mfma_f32_16x16x32_bf16 v[68:71], v[104:107], a[128:131], v[68:71]\n"
    "v_add_f32 v128, v128, v130\n"
    "v_mov_b32 v129, v128\n"
    "v_mfma_f32_16x16x32_bf16 v[76:79], a[124:127], v[108:111], v[76:79]\n"
    "v_permlane32_swap_b32 v128, v129\n"
    "v_mfma_f32_16x16x32_bf16 v[80:83], v[108:111], a[128:131], v[80:83]\n"
    "ds_read_b128 v[104:107], v63 offset:6656\n"
    "s_waitcnt lgkmcnt(3)\n"
    "v_mfma_f32_16x16x32_bf16 v[64:67], a[136:139], v[112:115], v[64:67]\n"
    "ds_read_b128 v[108:111], v4 offset:21504\n"
    "v_mul_f32 v131, s79, v128\n"
    "v_mfma_f32_16x16x32_bf16 v[72:75], a[132:135], v[116:119], v[72:75]\n"
    "v_mov_b32 v132, s81\n"
    "v_fma_f32 v132, v129, s80, v132\n"
    "v_mfma_f32_16x16x32_bf16 v[68:71], v[112:115], a[140:143], v[68:71]\n"
    "v_fma_f32 v132, -v131, v131, v132\n"
    "v_max_f32 v132, s81, v132\n"
    "v_mfma_f32_16x16x32_bf16 v[76:79], a[136:139], v[116:119], v[76:79]\n"
    "v_rsq_f32 v133, v132\n"
    "v_mfma_f32_16x16x32_bf16 v[80:83], v[116:119], a[140:143], v[80:83]\n"
    "ds_read_b128 v[112:115], v63 offset:7168\n"
    "s_mov_b64 exec, 0x7fffffff\n"
    "s_waitcnt vmcnt(1)\n"
    "v_cmp_ne_u32 vcc, s98, v152\n"
    "s_mov_b64 exec, -1\n"
    "s_cbranch_vccz .Lpd15\n"
    "s_mov_b64 exec, 2147483647\n"
    ".Lps15:\n"
    "s_sleep 1\n"
    "buffer_load_dword v152, v153, s[16:19], 0 offen sc1\n"
    "s_waitcnt vmcnt(0)\n"
    "v_cmp_ne_u32 vcc, s98, v152\n"
    "s_nop 1\n"
    "s_cbranch_vccz .Lpe15\n"
    "s_sub_u32 s67, s67, 1\n"
    "s_cbranch_scc0 .Lps15\n"
    "s_mov_b32 s67, 0\n"
    ".Lpe15:\n"
    "s_mov_b64 exec, -1\n"
    ".Lpd15:\n"
    "s_waitcnt lgkmcnt(3)\n"
    "v_mfma_f32_16x16x32_bf16 v[64:67], a[148:151], v[96:99], v[64:67]\n"
    "ds_read_b128 v[116:119], v4 offset:22528\n"
    "v_cvt_pk_bf16_f32 v136, v131, v131\n"
    "v_mfma_f32_16x16x32_bf16 v[72:75], a[144:147], v[100:103], v[72:75]\n"
    "v_lshlrev_b32 v134, 16, v136\n"
    "v_sub_f32 v134, v131, v134\n"
    "v_mfma_f32_16x16x32_bf16 v[68:71], v[96:99], a[152:155], v[68:71]\n"
    "v_cvt_pk_bf16_f32 v137, v134, 0\n"
    "v_add_u32 v147, s72, v150\n"
    "v_mfma_f32_16x16x32_bf16 v[76:79], a[148:151], v[100:103], v[76:79]\n"
    "ds_write_b64 v147, v[136:137] offset:2048\n"
    "v_mfma_f32_16x16x32_bf16 v[80:83], v[100:103], a[152:155], v[80:83]\n"
    "ds_read_b128 v[96:99], v63 offset:7680\n"
    "s_waitcnt lgkmcnt(4)\n"
    "v_mfma_f32_16x16x32_bf16 v[64:67], a[160:163], v[104:107], v[64:67]\n"
    "ds_read_b128 v[100:103], v4 offset:23552\n"
    "v_add_u32 v147, s74, v14\n"
    "v_mfma_f32_16x16x32_bf16 v[72:75], a[156:159], v[108:111], v[72:75]\n"
    "ds_write_b32 v147, v133 offset:3584\n"
    "v_mfma_f32_16x16x32_bf16 v[68:71], v[104:107], a[164:167], v[68:71]\n"
    "s_mov_b32 exec_hi, 0\n"
    "v_add_u32 v188, 0xc000, v188\n"
    "global_load_dwordx4 v[168:171], v188, s[16:17] sc1\n"
    "s_mov_b64 exec, -1\n"
    "v_mfma_f32_16x16x32_bf16 v[76:79], a[160:163], v[108:111], v[76:79]\n"
    "s_mov_b32 exec_hi, 0\n"
    "global_load_dwordx4 v[172:175], v188, s[16:17] offset:512 sc1\n"
    "s_mov_b64 exec, -1\n"
    "v_mfma_f32_16x16x32_bf16 v[80:83], v[108:111], a[164:167], v[80:83]\n"
    "s_waitcnt lgkmcnt(0)\n"
    "s_barrier\n"
    "v_mfma_f32_16x16x32_bf16 v[64:67], a[172:175], v[112:115], v[64:67]\n"
    "ds_read_b64 v[124:125], v15 offset:3072\n"
    "ds_read_b64 v[120:121], v15 offset:2048\n"
    "v_mfma_f32_16x16x32_bf16 v[72:75], a[168:171], v[116:119], v[72:75]\n"
    "ds_read_b64 v[122:123], v15 offset:2560\n"
    "ds_read_b128 v[104:107], v4 offset:24576\n"
    "v_mfma_f32_16x16x32_bf16 v[68:71], v[112:115], a[176:179], v[68:71]\n"
    "ds_read_b128 v[108:111], v4 offset:25600\n"
    "ds_read_b32 v144, v14 offset:3584\n"
    "v_mfma_f32_16x16x32_bf16 v[76:79], a[172:175], v[116:119], v[76:79]\n"
    "ds_read_b32 v145, v14 offset:3648\n"
    "ds_read_b32 v146, v14 offset:3712\n"
    "v_mfma_f32_16x16x32_bf16 v[80:83], v[116:119], a[176:179], v[80:83]\n"
    "ds_read_b128 v[112:115], v13 offset:3584\n"
    "ds_read_b128 v[116:119], v13 offset:3648\n"
    "v_mfma_f32_16x16x32_bf16 v[64:67], a[184:187], v[96:99], v[64:67]\n"
    "s_mov_b32 exec_hi, 0\n"
    "global_load_dwordx4 v[176:179], v188, s[16:17] offset:1024 sc1\n"
    "s_mov_b64 exec, -1\n"
    "s_mov_b32 s82, 0x1f10000\n"
    "s_mov_b64 exec, 0xff\n"
    "buffer_load_dword v60, v11, s[16:19], s82 offen sc1\n"
    "s_mov_b64 exec, -1\n"
    "v_mfma_f32_16x16x32_bf16 v[72:75], a[180:183], v[100:103], v[72:75]\n"
    "v_add_u32 v61, s39, v1\n"
    "global_load_dwordx4 a[192:195], v61, s[16:17] offset:-4096\n"
    "v_mfma_f32_16x16x32_bf16 v[68:71], v[96:99], a[188:191], v[68:71]\n"
    "global_load_dwordx4 a[196:199], v61, s[16:17] offset:-3072\n"
    "v_mfma_f32_16x16x32_bf16 v[76:79], a[184:187], v[100:103], v[76:79]\n"
    "v_mfma_f32_16x16x32_bf16 v[80:83], v[100:103], a[188:191], v[80:83]\n"
    "ds_read_b128 v[96:99], v4 offset:26624\n"
    "s_waitcnt lgkmcnt(1)\n"
    "v_mfma_f32_16x16x16_bf16 v[84:87], v[48:49], v[124:125], 0\n"
    "ds_read_b128 v[100:103], v4 offset:27648\n"
    "global_load_dwordx4 a[200:203], v61, s[16:17] offset:-2048\n"
    "v_mfma_f32_16x16x16_bf16 v[88:91], v[50:51], v[124:125], 0\n"
    "v_mfma_f32_16x16x16_bf16 v[92:95], v[124:125], v[52:53], 0\n"
    "v_mfma_f32_16x16x16_bf16 v[64:67], v[50:51], v[120:121], v[64:67]\n"
    "v_mfma_f32_16x16x16_bf16 v[72:75], v[48:49], v[122:123], v[72:75]\n"
    "global_load_dwordx4 a[204:207], v61, s[16:17] offset:-1024\n"
    "v_mfma_f32_16x16x16_bf16 v[76:79], v[50:51], v[122:123], v[76:79]\n"
    "v_mfma_f32_16x16x16_bf16 v[68:71], v[120:121], v[52:53], v[68:71]\n"
    "v_mfma_f32_16x16x16_bf16 v[80:83], v[122:123], v[52:53], v[80:83]\n"
    "v_mul_f32 v64, v64, v144\n"
    "v_mul_f32 v65, v65, v144\n"
    "v_mfma_f32_16x16x32_bf16 v[84:87], a[0:3], v[104:107], v[84:87]\n"
    "v_mul_f32 v66, v66, v144\n"
    "v_mul_f32 v67, v67, v144\n"
    "v_mfma_f32_16x16x32_bf16 v[88:91], a[4:7], v[104:107], v[88:91]\n"
    "v_cvt_pk_bf16_f32 v126, v64, v65\n"
    "v_cvt_pk_bf16_f32 v127, v66, v67\n"
    "v_mfma_f32_16x16x32_bf16 v[92:95], v[104:107], a[8:11], v[92:95]\n"
    "ds_read_b128 v[104:107], v4 offset:28672\n"
    "v_cvt_pk_bf16_f32 v132, v72, v73\n"
    "v_mfma_f32_16x16x32_bf16 v[84:87], a[12:15], v[108:111], v[84:87]\n"
    "v_cvt_pk_bf16_f32 v133, v74, v75\n"
    "v_mul_f32 v76, v76, v145\n"
    "v_mfma_f32_16x16x32_bf16 v[88:91], a[16:19], v[108:111], v[88:91]\n"
    "v_mul_f32 v77, v77, v145\n"
    "v_mul_f32 v78, v78, v145\n"
    "v_mfma_f32_16x16x32_bf16 v[92:95], v[108:111], a[20:23], v[92:95]\n"
    "ds_read_b128 v[108:111], v4 offset:29696\n"
    "s_waitcnt lgkmcnt(2)\n"
    "v_mfma_f32_16x16x32_bf16 v[84:87], a[24:27], v[96:99], v[84:87]\n"
    "v_mul_f32 v79, v79, v145\n"
    "v_cvt_pk_bf16_f32 v128, v76, v77\n"
    "v_mfma_f32_16x16x32_bf16 v[88:91], a[28:31], v[96:99], v[88:91]\n"
    "v_cvt_pk_bf16_f32 v129, v78, v79\n"
    "v_mul_f32 v68, v68, v112\n"
    "v_mfma_f32_16x16x32_bf16 v[92:95], v[96:99], a[32:35], v[92:95]\n"
    "ds_read_b128 v[96:99], v4 offset:30720\n"
    "v_mul_f32 v69, v69, v113\n"
    "v_mfma_f32_16x16x32_bf16 v[84:87], a[36:39], v[100:103], v[84:87]\n"
    "v_mul_f32 v70, v70, v114\n"
    "v_mul_f32 v71, v71, v115\n"
    "v_mfma_f32_16x16x32_bf16 v[88:91], a[40:43], v[100:103], v[88:91]\n"
    "v_cvt_pk_bf16_f32 v120, v68, v69\n"
    "v_cvt_pk_bf16_f32 v121, v70, v71\n"
    "v_mfma_f32_16x16x32_bf16 v[92:95], v[100:103], a[44:47], v[92:95]\n"
    "ds_read_b128 v[100:103], v4 offset:31744\n"
    "ds_read_b128 v[112:115], v13 offset:3712\n"
    "v_mul_f32 v80, v80, v116\n"
    "s_waitcnt lgkmcnt(4)\n"
    "v_mfma_f32_16x16x32_bf16 v[84:87], a[48:51], v[104:107], v[84:87]\n"
    "v_mul_f32 v81, v81, v117\n"
    "v_mul_f32 v82, v82, v118\n"
    "v_mfma_f32_16x16x32_bf16 v[88:91], a[52:55], v[104:107], v[88:91]\n"
    "v_mul_f32 v83, v83, v119\n"
    "v_cvt_pk_bf16_f32 v122, v80, v81\n"
    "v_mfma_f32_16x16x32_bf16 v[92:95], v[104:107], a[56:59], v[92:95]\n"
    "ds_read_b128 v[104:107], v4 offset:32768\n"
    "v_cvt_pk_bf16_f32 v123, v82, v83\n"
    "s_waitcnt lgkmcnt(4)\n"
    "v_mfma_f32_16x16x32_bf16 v[84:87], a[60:63], v[108:111], v[84:87]\n"
    "v_mfma_f32_16x16x16_bf16 v[64:67], v[126:127], v[132:133], v[32:35]\n"
    "v_mfma_f32_16x16x16_bf16 v[68:71], v[128:129], v[132:133], v[40:43]\n"
    "v_mul_f32 v142, s76, v145\n"
    "global_load_dwordx4 a[208:211], v61, s[16:17]\n"
    "v_mfma_f32_16x16x32_bf16 v[88:91], a[64:67], v[108:111], v[88:91]\n"
    "v_max3_f32 v140, v64, v65, v66\n"
    "s_nop 2\n"
    "v_max3_f32 v141, v67, v68, v69\n"
    "v_mfma_f32_16x16x32_bf16 v[92:95], v[108:111], a[68:71], v[92:95]\n"
    "ds_read_b128 v[108:111], v4 offset:33792\n"
    "s_waitcnt lgkmcnt(2)\n"
    "v_mfma_f32_16x16x32_bf16 v[84:87], a[72:75], v[96:99], v[84:87]\n"
    "v_max3_f32 v140, v140, v70, v71\n"
    "v_max_f32 v140, v140, v141\n"
    "v_mfma_f32_16x16x32_bf16 v[88:91], a[76:79], v[96:99], v[88:91]\n"
    "v_mov_b32 v141, v140\n"
    "global_load_dwordx4 a[212:215], v61, s[16:17] offset:1024\n"
    "v_mfma_f32_16x16x32_bf16 v[92:95], v[96:99], a[80:83], v[92:95]\n"
    "ds_read_b128 v[96:99], v4 offset:34816\n"
    "v_permlane16_swap_b32 v141, v140\n"
    "v_mfma_f32_16x16x32_bf16 v[84:87], a[84:87], v[100:103], v[84:87]\n"
    "v_max_f32 v140, v140, v141\n"
    "v_mov_b32 v141, v140\n"
    "v_mfma_f32_16x16x32_bf16 v[88:91], a[88:91], v[100:103], v[88:91]\n"
    "v_permlane32_swap_b32 v141, v140\n"
    "v_mfma_f32_16x16x32_bf16 v[92:95], v[100:103], a[92:95], v[92:95]\n"
    "ds_read_b128 v[100:103], v4 offset:35840\n"
    "s_waitcnt lgkmcnt(3)\n"
    "v_mfma_f32_16x16x32_bf16 v[84:87], a[96:99], v[104:107], v[84:87]\n"
    "v_max_f32 v140, v140, v141\n"
    "v_mul_f32 v140, v142, v140\n"
    "v_mfma_f32_16x16x32_bf16 v[88:91], a[100:103], v[104:107], v[88:91]\n"
    "v_fma_f32 v64, v64, v142, -v140\n"
    "v_fma_f32 v65, v65, v142, -v140\n"
    "v_mfma_f32_16x16x32_bf16 v[92:95], v[104:107], a[104:107], v[92:95]\n"
    "ds_read_b128 v[104:107], v4 offset:36864\n"
    "v_fma_f32 v66, v66, v142, -v140\n"
    "s_waitcnt lgkmcnt(3)\n"
    "v_mfma_f32_16x16x32_bf16 v[84:87], a[108:111], v[108:111], v[84:87]\n"
    "v_fma_f32 v67, v67, v142, -v140\n"
    "v_fma_f32 v68, v68, v142, -v140\n"
    "v_mfma_f32_16x16x32_bf16 v[88:91], a[112:115], v[108:111], v[88:91]\n"
    "v_fma_f32 v69, v69, v142, -v140\n"
    "v_fma_f32 v70, v70, v142, -v140\n"
    "v_mfma_f32_16x16x32_bf16 v[92:95], v[108:111], a[116:119], v[92:95]\n"
    "ds_read_b128 v[108:111], v4 offset:37888\n"
    "v_fma_f32 v71, v71, v142, -v140\n"
    "v_exp_f32 v64, v64\n"
    "s_waitcnt lgkmcnt(2)\n"
    "v_mfma_f32_16x16x32_bf16 v[84:87], a[120:123], v[96:99], v[84:87]\n"
    "v_exp_f32 v65, v65\n"
    "v_mfma_f32_16x16x32_bf16 v[88:91], a[124:127], v[96:99], v[88:91]\n"
    "v_exp_f32 v66, v66\n"
    "v_mfma_f32_16x16x32_bf16 v[92:95], v[96:99], a[128:131], v[92:95]\n"
    "ds_read_b128 v[96:99], v4 offset:38912\n"
    "global_load_dwordx4 a[216:219], v61, s[16:17] offset:2048\n"
    "v_mfma_f32_16x16x32_bf16 v[84:87], a[132:135], v[100:103], v[84:87]\n"
    "v_exp_f32 v67, v67\n"
    "v_mfma_f32_16x16x32_bf16 v[88:91], a[136:139], v[100:103], v[88:91]\n"
    "v_exp_f32 v68, v68\n"
    "v_mfma_f32_16x16x32_bf16 v[92:95], v[100:103], a[140:143], v[92:95]\n"
    "ds_read_b128 v[100:103], v4 offset:39936\n"
    "v_exp_f32 v69, v69\n"
    "s_waitcnt lgkmcnt(2)\n"
    "v_mfma_f32_16x16x32_bf16 v[84:87], a[144:147], v[104:107], v[84:87]\n"
    "v_exp_f32 v70, v70\n"
    "v_mfma_f32_16x16x32_bf16 v[88:91], a[148:151], v[104:107], v[88:91]\n"
    "v_exp_f32 v71, v71\n"
    "v_mfma_f32_16x16x32_bf16 v[92:95], v[104:107], a[152:155], v[92:95]\n"
    "s_mov_b64 exec, 0xff\n"
    "s_waitcnt vmcnt(7)\n"
    "v_cmp_ne_u32 vcc, s98, v60\n"
    "s_mov_b64 exec, -1\n"
    "s_cbranch_vccz .Lpd1\n"
    "s_mov_b64 exec, 255\n"
    ".Lps1:\n"
    "s_sleep 1\n"
    "buffer_load_dword v60, v11, s[16:19], s82 offen sc1\n"
    "s_waitcnt vmcnt(0)\n"
    "v_cmp_ne_u32 vcc, s98, v60\n"
    "s_nop 1\n"
    "s_cbranch_vccz .Lpe1\n"
    "s_sub_u32 s67, s67, 1\n"
    "s_cbranch_scc0 .Lps1\n"
    "s_mov_b32 s67, 0\n"
    ".Lpe1:\n"
    "s_mov_b64 exec, -1\n"
    ".Lpd1:\n"
    "s_add_u32 m0, s51, 0\n"
    "s_add_u32 s87, s36, 0x780000\n"
    "v_add_u32 v62, s87, v1\n"
    "global_load_lds_dwordx4 v62, s[16:17] sc1\n"
    "global_load_lds_dwordx4 v62, s[16:17] offset:1024 sc1\n"
    "global_load_lds_dwordx4 v62, s[16:17] offset:2048 sc1\n"
    "global_load_lds_dwordx4 v62, s[16:17] offset:3072 sc1\n"
    "v_cvt_pk_bf16_f32 v72, v64, v65\n"
    "v_mfma_f32_16x16x32_bf16 v[84:87], a[156:159], v[108:111], v[84:87]\n"
    "v_cvt_pk_bf16_f32 v73, v66, v67\n"
    "v_cvt_pk_bf16_f32 v74, v68, v69\n"
    "v_mfma_f32_16x16x32_bf16 v[88:91], a[160:163], v[108:111], v[88:91]\n"
    "v_cvt_pk_bf16_f32 v75, v70, v71\n"
    "v_mfma_f32_16x16x32_bf16 v[92:95], v[108:111], a[164:167], v[92:95]\n"
    "v_mfma_f32_16x16x32_bf16 v[80:83], v[44:47], v[72:75], 0\n"
    "s_waitcnt lgkmcnt(0)\n"
    "v_mfma_f32_16x16x32_bf16 v[84:87], a[168:171], v[96:99], v[84:87]\n"
    "v_mfma_f32_16x16x32_bf16 v[76:79], v[120:123], v[72:75], 0\n"
    "v_rcp_f32 v141, v80\n"
    "v_mfma_f32_16x16x32_bf16 v[88:91], a[172:175], v[96:99], v[88:91]\n"
    "s_add_u32 m0, s51, 0x1000\n"
    "s_add_u32 s87, s36, 0x781000\n"
    "v_add_u32 v62, s87, v1\n"
    "global_load_lds_dwordx4 v62, s[16:17] sc1\n"
    "global_load_lds_dwordx4 v62, s[16:17] offset:1024 sc1\n"
    "global_load_lds_dwordx4 v62, s[16:17] offset:2048 sc1\n"
    "global_load_lds_dwordx4 v62, s[16:17] offset:3072 sc1\n"
    "v_mfma_f32_16x16x32_bf16 v[92:95], v[96:99], a[176:179], v[92:95]\n"
    "v_mul_f32 v76, v76, v141\n"
    "v_mul_f32 v77, v77, v141\n"
    "v_mfma_f32_16x16x32_bf16 v[84:87], a[180:183], v[100:103], v[84:87]\n"
    "v_mul_f32 v78, v78, v141\n"
    "v_mul_f32 v79, v79, v141\n"
    "v_mfma_f32_16x16x32_bf16 v[88:91], a[184:187], v[100:103], v[88:91]\n"
    "v_cvt_pk_bf16_f32 v136, v76, v77\n"
    "v_cvt_pk_bf16_f32 v137, v78, v79\n"
    "v_mfma_f32_16x16x32_bf16 v[92:95], v[100:103], a[188:191], v[92:95]\n"
    "s_nop 3\n"
    "v_mul_f32 v88, v88, v146\n"
    "v_mul_f32 v89, v89, v146\n"
    "v_mul_f32 v90, v90, v146\n"
    "v_mul_f32 v91, v91, v146\n"
    "v_cvt_pk_bf16_f32 v130, v88, v89\n"
    "v_cvt_pk_bf16_f32 v131, v90, v91\n"
    "v_cvt_pk_bf16_f32 v134, v84, v85\n"
    "v_cvt_pk_bf16_f32 v135, v86, v87\n"
    "s_nop 1\n"
    "v_mfma_f32_16x16x16_bf16 v[64:67], v[128:129], v[134:135], v[36:39]\n"
    "v_mfma_f32_16x16x16_bf16 v[68:71], v[130:131], v[134:135], v[40:43]\n"
    "v_mul_f32 v142, s76, v146\n"
    "v_mul_f32 v92, v92, v112\n"
    "v_mul_f32 v93, v93, v113\n"
    "v_mul_f32 v94, v94, v114\n"
    "v_mul_f32 v95, v95, v115\n"
    "v_cvt_pk_bf16_f32 v124, v92, v93\n"
    "v_cvt_pk_bf16_f32 v125, v94, v95\n"
    "v_max3_f32 v140, v64, v65, v66\n"
    "v_max3_f32 v141, v67, v68, v69\n"
    "v_max3_f32 v140, v140, v70, v71\n"
    "v_max_f32 v140, v140, v141\n"
    "v_mov_b32 v141, v140\n"
    "s_nop 1\n"
    "v_permlane16_swap_b32 v141, v140\n"
    "v_max_f32 v140, v140, v141\n"
    "global_load_dwordx4 a[220:223], v61, s[16:17] offset:3072\n"
    "v_mov_b32 v141, v140\n"
    "v_add_u32 v61, 0x2000, v61\n"
    "global_load_dwordx4 a[224:227], v61, s[16:17] offset:-4096\n"
    "global_load_dwordx4 a[228:231], v61, s[16:17] offset:-3072\n"
    "v_permlane32_swap_b32 v141, v140\n"
    "global_load_dwordx4 a[232:235], v61, s[16:17] offset:-2048\n"
    "global_load_dwordx4 a[236:239], v61, s[16:17] offset:-1024\n"
    "v_max_f32 v140, v140, v141\n"
    "global_load_dwordx4 a[240:243], v61, s[16:17]\n"
    "global_load_dwordx4 a[244:247], v61, s[16:17] offset:1024\n"
    "v_mul_f32 v140, v142, v140\n"
    "global_load_dwordx4 a[248:251], v61, s[16:17] offset:2048\n"
    "global_load_dwordx4 a[252:255], v61, s[16:17] offset:3072\n"
    "v_fma_f32 v64, v64, v142, -v140\n"
    "v_fma_f32 v65, v65, v142, -v140\n"
    "v_fma_f32 v66, v66, v142, -v140\n"
    "v_fma_f32 v67, v67, v142, -v140\n"
    "v_fma_f32 v68, v68, v142, -v140\n"
    "v_fma_f32 v69, v69, v142, -v140\n"
    "v_fma_f32 v70, v70, v142, -v140\n"
    "v_fma_f32 v71, v71, v142, -v140\n"
    "v_exp_f32 v64, v64\n"
    "v_exp_f32 v65, v65\n"
    "v_exp_f32 v66, v66\n"
    "v_exp_f32 v67, v67\n"
    "v_exp_f32 v68, v68\n"
    "v_exp_f32 v69, v69\n"
    "v_exp_f32 v70, v70\n"
    "v_exp_f32 v71, v71\n"
    "v_cvt_pk_bf16_f32 v72, v64, v65\n"
    "v_cvt_pk_bf16_f32 v73, v66, v67\n"
    "v_cvt_pk_bf16_f32 v74, v68, v69\n"
    "v_cvt_pk_bf16_f32 v75, v70, v71\n"
    "s_nop 1\n"
    "v_mfma_f32_16x16x32_bf16 v[80:83], v[44:47], v[72:75], 0\n"
    "v_mfma_f32_16x16x32_bf16 v[76:79], v[122:125], v[72:75], 0\n"
    "s_nop 3\n"
    "v_rcp_f32 v141, v80\n"
    "s_nop 2\n"
    "v_mul_f32 v76, v76, v141\n"
    "v_mul_f32 v77, v77, v141\n"
    "v_mul_f32 v78, v78, v141\n"
    "v_mul_f32 v79, v79, v141\n"
    "v_cvt_pk_bf16_f32 v138, v76, v77\n"
    "v_cvt_pk_bf16_f32 v139, v78, v79\n"
    "s_nop 0\n"
    "v_permlane16_swap_b32 v136, v138\n"
    "v_permlane16_swap_b32 v137, v139\n"
    "s_add_u32 s84, s44, 0x788000\n"
    "buffer_store_dwordx4 v[136:139], v1, s[16:19], s84 offen sc1\n"
    "v_add_f32 v168, v168, v169\n"
    "v_add_f32 v170, v170, v171\n"
    "v_add_f32 v168, v168, v170\n"
    "v_cvt_pk_bf16_f32 v55, v168, 0\n"
    "v_lshlrev_b32 v189, 16, v55\n"
    "v_sub_f32 v189, v168, v189\n"
    "v_cvt_pk_bf16_f32 v54, v168, v189\n"
    "v_add_f32 v172, v172, v173\n"
    "v_add_f32 v174, v174, v175\n"
    "v_add_f32 v172, v172, v174\n"
    "v_cvt_pk_bf16_f32 v57, v172, 0\n"
    "v_lshlrev_b32 v189, 16, v57\n"
    "v_sub_f32 v189, v172, v189\n"
    "v_cvt_pk_bf16_f32 v56, v172, v189\n"
    "v_add_f32 v176, v176, v177\n"
    "v_add_f32 v178, v178, v179\n"
    "v_add_f32 v176, v176, v178\n"
    "v_cvt_pk_bf16_f32 v59, v176, 0\n"
    "v_lshlrev_b32 v189, 16, v59\n"
    "v_sub_f32 v189, v176, v189\n"
    "v_cvt_pk_bf16_f32 v58, v176, v189\n"
    "s_waitcnt vmcnt(14)\n"
    "s_add_u32 s63, s51, 0\n"
    "v_add_u32 v5, s63, v1\n"
    "ds_read_b128 v[96:99], v5 offset:0\n"
    "ds_read_b128 v[100:103], v5 offset:1024\n"
    "ds_read_b128 v[104:107], v5 offset:2048\n"
    "ds_read_b128 v[108:111], v5 offset:3072\n"
    "s_add_u32 s64, s54, 0x1000\n"
    "v_add_u32 v120, s64, v1\n"
    "s_add_u32 s64, s55, 0\n"
    "v_add_u32 v121, s64, v1\n"
    "s_add_u32 s64, s56, 0x800\n"
    "v_add_u32 v122, s64, v1\n"
    "v_add_u32 v61, s40, v1\n"
    "global_load_dwordx4 v[152:155], v61, s[16:17] offset:-4096\n"
    "global_load_dwordx4 v[156:159], v61, s[16:17] offset:-3072\n"
    "s_waitcnt lgkmcnt(2)\n"
    "v_mfma_f32_32x32x16_bf16 v[64:79], a[192:195], v[96:99], 0\n"
    "global_load_dwordx4 v[160:163], v61, s[16:17] offset:-2048\n"
    "v_mfma_f32_32x32x16_bf16 v[80:95], a[196:199], v[96:99], 0\n"
    "global_load_dwordx4 v[164:167], v61, s[16:17] offset:-1024\n"
    "v_mfma_f32_32x32x16_bf16 v[64:79], a[200:203], v[100:103], v[64:79]\n"
    "v_mfma_f32_32x32x16_bf16 v[80:95], a[204:207], v[100:103], v[80:95]\n"
    "global_load_dwordx4 v[168:171], v61, s[16:17]\n"
    "s_waitcnt lgkmcnt(0)\n"
    "v_mfma_f32_32x32x16_bf16 v[64:79], a[208:211], v[104:107], v[64:79]\n"
    "s_waitcnt vmcnt(15)\n"
    "ds_read_b128 v[112:115], v5 offset:4096\n"
    "v_mfma_f32_32x32x16_bf16 v[80:95], a[212:215], v[104:107], v[80:95]\n"
    "ds_read_b128 v[116:119], v5 offset:5120\n"
    "ds_read_b128 v[96:99], v5 offset:6144\n"
    "ds_read_b128 v[100:103], v5 offset:7168\n"
    "global_load_dwordx4 v[172:175], v61, s[16:17] offset:1024\n"
    "v_mfma_f32_32x32x16_bf16 v[64:79], a[216:219], v[108:111], v[64:79]\n"
    "s_waitcnt vmcnt(15)\n"
    "v_mfma_f32_32x32x16_bf16 v[80:95], a[220:223], v[108:111], v[80:95]\n"
    "global_load_dwordx4 v[176:179], v61, s[16:17] offset:2048\n"
    "global_load_dwordx4 v[180:183], v61, s[16:17] offset:3072\n"
    "s_waitcnt vmcnt(15)\n"
    "s_waitcnt lgkmcnt(0)\n"
    "v_mfma_f32_32x32x16_bf16 v[80:95], a[228:231], v[112:115], v[80:95]\n"
    "v_add_u32 v61, 0x2000, v61\n"
    "global_load_dwordx4 v[184:187], v61, s[16:17] offset:-4096\n"
    "global_load_dwordx4 v[188:191], v61, s[16:17] offset:-3072\n"
    "s_waitcnt vmcnt(15)\n"
    "v_mfma_f32_32x32x16_bf16 v[80:95], a[236:239], v[116:119], v[80:95]\n"
    "global_load_dwordx4 v[192:195], v61, s[16:17] offset:-2048\n"
    "global_load_dwordx4 v[196:199], v61, s[16:17] offset:-1024\n"
    "s_waitcnt vmcnt(15)\n"
    "v_mfma_f32_32x32x16_bf16 v[80:95], a[244:247], v[96:99], v[80:95]\n"
    "global_load_dwordx4 v[200:203], v61, s[16:17]\n"
    "global_load_dwordx4 v[204:207], v61, s[16:17] offset:1024\n"
    "s_waitcnt vmcnt(14)\n"
    "v_mfma_f32_32x32x16_bf16 v[80:95], a[252:255], v[100:103], v[80:95]\n"
    "s_barrier\n"
    "v_mov_b32 v62, s98\n"
    "s_add_u32 s83, s47, 0x1f10080\n"
    "s_mov_b64 exec, s[68:69]\n"
    "buffer_store_dword v62, off, s[16:19], s83 sc1\n"
    "s_mov_b64 exec, -1\n"
    "v_mfma_f32_32x32x16_bf16 v[64:79], a[224:227], v[112:115], v[64:79]\n"
    "s_nop 3\n"
    "ds_write_b128 v5, v[80:83] offset:0\n"
    "v_mfma_f32_32x32x16_bf16 v[64:79], a[232:235], v[116:119], v[64:79]\n"
    "global_load_dwordx4 v[208:211], v61, s[16:17] offset:2048\n"
    "ds_write_b128 v5, v[84:87] offset:1024\n"
    "v_mfma_f32_32x32x16_bf16 v[64:79], a[240:243], v[96:99], v[64:79]\n"
    "v_mfma_f32_32x32x16_bf16 v[64:79], a[248:251], v[100:103], v[64:79]\n"
    "ds_write_b128 v5, v[88:91] offset:2048\n"
    "ds_write_b128 v5, v[92:95] offset:3072\n"
    "global_load_dwordx4 v[212:215], v61, s[16:17] offset:3072\n"
    "s_nop 7\n"
    "s_nop 0\n"
    "ds_write_b128 v5, v[72:75] offset:4096\n"
    "ds_write_b128 v5, v[76:79] offset:5120\n"
    "v_add_f32 v16, v16, v64\n"
    "v_add_f32 v17, v17, v65\n"
    "v_add_f32 v18, v18, v66\n"
    "v_add_f32 v19, v19, v67\n"
    "v_add_f32 v20, v20, v68\n"
    "v_add_f32 v21, v21, v69\n"
    "v_add_f32 v22, v22, v70\n"
    "v_add_f32 v23, v23, v71\n"
    "s_waitcnt lgkmcnt(0)\n"
    "s_barrier\n"
    "ds_read_b128 v[104:107], v121 offset:0\n"
    "ds_read_b128 v[108:111], v121 offset:1024\n"
    "ds_read_b128 v[112:115], v122 offset:0\n"
    "ds_read_b128 v[116:119], v122 offset:1024\n"
    "ds_read_b128 v[96:99], v120 offset:0\n"
    "ds_read_b128 v[100:103], v120 offset:1024\n"
    "v_add_u32 v61, 0x2000, v61\n"
    "global_load_dwordx4 v[216:219], v61, s[16:17] offset:-4096\n"
    "global_load_dwordx4 v[220:223], v61, s[16:17] offset:-3072\n"
    "global_load_dwordx4 v[224:227], v61, s[16:17] offset:-2048\n"
    "global_load_dwordx4 v[228:231], v61, s[16:17] offset:-1024\n"
    "s_waitcnt lgkmcnt(2)\n"
    "v_add_f32 v104, v104, v112\n"
    "v_add_f32 v105, v105, v113\n"
    "v_add_f32 v106, v106, v114\n"
    "v_add_f32 v107, v107, v115\n"
    "v_add_f32 v108, v108, v116\n"
    "v_add_f32 v109, v109, v117\n"
    "v_add_f32 v110, v110, v118\n"
    "v_add_f32 v111, v111, v119\n"
    "s_waitcnt lgkmcnt(0)\n"
    "v_add_f32 v16, v16, v96\n"
    "v_add_f32 v17, v17, v97\n"
    "v_add_f32 v18, v18, v98\n"
    "v_add_f32 v19, v19, v99\n"
    "v_add_f32 v20, v20, v100\n"
    "v_add_f32 v21, v21, v101\n"
    "v_add_f32 v22, v22, v102\n"
    "v_add_f32 v23, v23, v103\n"
    "v_add_f32 v16, v16, v104\n"
    "v_add_f32 v17, v17, v105\n"
    "v_add_f32 v18, v18, v106\n"
    "v_add_f32 v19, v19, v107\n"
    "v_add_f32 v20, v20, v108\n"
    "v_add_f32 v21, v21, v109\n"
    "v_add_f32 v22, v22, v110\n"
    "v_add_f32 v23, v23, v111\n"
    "v_cvt_pk_bf16_f32 v124, v16, v17\n"
    "v_cvt_pk_bf16_f32 v125, v18, v19\n"
    "v_cvt_pk_bf16_f32 v126, v20, v21\n"
    "v_cvt_pk_bf16_f32 v127, v22, v23\n"
    "s_add_u32 s84, s42, 0x980000\n"
    "buffer_store_dwordx4 v[124:127], v8, s[16:19], s84 offen sc1\n"
    "v_add_f32 v128, v16, v17\n"
    "v_mul_f32 v129, v16, v16\n"
    "v_add_f32 v130, v18, v19\n"
    "v_mul_f32 v131, v17, v17\n"
    "v_fma_f32 v129, v18, v18, v129\n"
    "v_fma_f32 v131, v19, v19, v131\n"
    "v_add_f32 v128, v128, v20\n"
    "v_add_f32 v130, v130, v21\n"
    "v_fma_f32 v129, v20, v20, v129\n"
    "v_fma_f32 v131, v21, v21, v131\n"
    "v_add_f32 v128, v128, v22\n"
    "v_add_f32 v130, v130, v23\n"
    "v_fma_f32 v129, v22, v22, v129\n"
    "v_fma_f32 v131, v23, v23, v131\n"
    "s_mov_b32 s82, 0x1f10080\n"
    "s_mov_b64 exec, 0xff\n"
    "buffer_load_dword v60, v11, s[16:19], s82 offen sc1\n"
    "s_mov_b64 exec, -1\n"
    "v_add_f32 v128, v128, v130\n"
    "v_add_f32 v129, v129, v131\n"
    "s_add_u32 s85, s43, 0xb80000\n"
    "global_load_dwordx4 v[232:235], v61, s[16:17]\n"
    "v_permlane32_swap_b32 v128, v129\n"
    "v_add_f32 v128, v128, v129\n"
    "buffer_store_dword v128, v9, s[16:19], s85 offen sc1\n"
    "global_load_dwordx4 v[236:239], v61, s[16:17] offset:1024\n"
    "global_load_dwordx4 v[240:243], v61, s[16:17] offset:2048\n"
    "global_load_dwordx4 v[244:247], v61, s[16:17] offset:3072\n"
    "v_add_u32 v61, 0x2000, v61\n"
    "global_load_dwordx4 v[248:251], v61, s[16:17] offset:-4096\n"
    "global_load_dwordx4 v[252:255], v61, s[16:17] offset:-3072\n"
    "global_load_dwordx4 a[0:3], v61, s[16:17] offset:-2048\n"
    "global_load_dwordx4 a[4:7], v61, s[16:17] offset:-1024\n"
    "global_load_dwordx4 a[8:11], v61, s[16:17]\n"
    "global_load_dwordx4 a[12:15], v61, s[16:17] offset:1024\n"
    "global_load_dwordx4 a[16:19], v61, s[16:17] offset:2048\n"
    "global_load_dwordx4 a[20:23], v61, s[16:17] offset:3072\n"
    "v_add_u32 v61, 0x2000, v61\n"
    "global_load_dwordx4 a[24:27], v61, s[16:17] offset:-4096\n"
    "global_load_dwordx4 a[28:31], v61, s[16:17] offset:-3072\n"
    "global_load_dwordx4 a[32:35], v61, s[16:17] offset:-2048\n"
    "s_waitcnt vmcnt(14)\n"
    "s_barrier\n"
    "v_mov_b32 v62, s98\n"
    "s_add_u32 s83, s47, 0x1f12000\n"
    "s_mov_b64 exec, s[68:69]\n"
    "buffer_store_dword v62, off, s[16:19], s83 sc1\n"
    "s_mov_b64 exec, -1\n"
    "s_mov_b64 exec, 0xff\n"
    "v_cmp_ne_u32 vcc, s98, v60\n"
    "s_mov_b64 exec, -1\n"
    "s_cbranch_vccz .Lpd2\n"
    "s_mov_b64 exec, 255\n"
    ".Lps2:\n"
    "s_sleep 1\n"
    "buffer_load_dword v60, v11, s[16:19], s82 offen sc1\n"
    "s_waitcnt vmcnt(0)\n"
    "v_cmp_ne_u32 vcc, s98, v60\n"
    "s_nop 1\n"
    "s_cbranch_vccz .Lpe2\n"
    "s_sub_u32 s67, s67, 1\n"
    "s_cbranch_scc0 .Lps2\n"
    "s_mov_b32 s67, 0\n"
    ".Lpe2:\n"
    "s_mov_b64 exec, -1\n"
    ".Lpd2:\n"
    "s_add_u32 m0, s51, 0x8000\n"
    "s_add_u32 s87, s36, 0x788000\n"
    "v_add_u32 v62, s87, v1\n"
    "global_load_lds_dwordx4 v62, s[16:17] sc1\n"
    "global_load_lds_dwordx4 v62, s[16:17] offset:1024 sc1\n"
    "global_load_lds_dwordx4 v62, s[16:17] offset:2048 sc1\n"
    "global_load_lds_dwordx4 v62, s[16:17] offset:3072 sc1\n"
    "s_add_u32 m0, s51, 0x9000\n"
    "s_add_u32 s87, s36, 0x789000\n"
    "v_add_u32 v62, s87, v1\n"
    "global_load_lds_dwordx4 v62, s[16:17] sc1\n"
    "global_load_lds_dwordx4 v62, s[16:17] offset:1024 sc1\n"
    "global_load_lds_dwordx4 v62, s[16:17] offset:2048 sc1\n"
    "global_load_lds_dwordx4 v62, s[16:17] offset:3072 sc1\n"
    "global_load_dwordx4 a[36:39], v61, s[16:17] offset:-1024\n"
    "global_load_dwordx4 a[40:43], v61, s[16:17]\n"
    "s_mov_b32 s82, 0x1f12000\n"
    "s_mov_b64 exec, 0xff\n"
    "buffer_load_dword v60, v11, s[16:19], s82 offen sc1\n"
    "s_mov_b64 exec, -1\n"
    "global_load_dwordx4 a[44:47], v61, s[16:17] offset:1024\n"
    "global_load_dwordx4 a[48:51], v61, s[16:17] offset:2048\n"
    "global_load_dwordx4 a[52:55], v61, s[16:17] offset:3072\n"
    "v_add_u32 v61, 0x2000, v61\n"
    "global_load_dwordx4 a[56:59], v61, s[16:17] offset:-4096\n"
    "global_load_dwordx4 a[60:63], v61, s[16:17] offset:-3072\n"
    "global_load_dwordx4 a[64:67], v61, s[16:17] offset:-2048\n"
    "global_load_dwordx4 a[68:71], v61, s[16:17] offset:-1024\n"
    "s_waitcnt vmcnt(14)\n"
    "s_add_u32 s63, s51, 0x8000\n"
    "v_add_u32 v5, s63, v1\n"
    "ds_read_b128 v[96:99], v5 offset:0\n"
    "ds_read_b128 v[100:103], v5 offset:1024\n"
    "ds_read_b128 v[104:107], v5 offset:2048\n"
    "ds_read_b128 v[108:111], v5 offset:3072\n"
    "s_add_u32 s64, s54, 0x9000\n"
    "v_add_u32 v120, s64, v1\n"
    "s_add_u32 s64, s55, 0x8000\n"
    "v_add_u32 v121, s64, v1\n"
    "s_add_u32 s64, s56, 0x8800\n"
    "v_add_u32 v122, s64, v1\n"
    "global_load_dwordx4 a[72:75], v61, s[16:17]\n"
    "global_load_dwordx4 a[76:79], v61, s[16:17] offset:1024\n"
    "s_waitcnt lgkmcnt(2)\n"
    "v_mfma_f32_32x32x16_bf16 v[64:79], a[192:195], v[96:99], 0\n"
    "global_load_dwordx4 a[80:83], v61, s[16:17] offset:2048\n"
    "v_mfma_f32_32x32x16_bf16 v[80:95], a[196:199], v[96:99], 0\n"
    "global_load_dwordx4 a[84:87], v61, s[16:17] offset:3072\n"
    "v_mfma_f32_32x32x16_bf16 v[64:79], a[200:203], v[100:103], v[64:79]\n"
    "v_mfma_f32_32x32x16_bf16 v[80:95], a[204:207], v[100:103], v[80:95]\n"
    "s_waitcnt lgkmcnt(0)\n"
    "v_mfma_f32_32x32x16_bf16 v[64:79], a[208:211], v[104:107], v[64:79]\n"
    "v_add_u32 v61, s41, v1\n"
    "global_load_dwordx4 a[88:91], v61, s[16:17] offset:-4096\n"
    "s_waitcnt vmcnt(15)\n"
    "ds_read_b128 v[112:115], v5 offset:4096\n"
    "v_mfma_f32_32x32x16_bf16 v[80:95], a[212:215], v[104:107], v[80:95]\n"
    "ds_read_b128 v[116:119], v5 offset:5120\n"
    "ds_read_b128 v[96:99], v5 offset:6144\n"
    "ds_read_b128 v[100:103], v5 offset:7168\n"
    "global_load_dwordx4 a[92:95], v61, s[16:17] offset:-3072\n"
    "v_mfma_f32_32x32x16_bf16 v[64:79], a[216:219], v[108:111], v[64:79]\n"
    "v_mfma_f32_32x32x16_bf16 v[80:95], a[220:223], v[108:111], v[80:95]\n"
    "global_load_dwordx4 a[96:99], v61, s[16:17] offset:-2048\n"
    "s_waitcnt lgkmcnt(3)\n"
    "v_mfma_f32_32x32x16_bf16 v[80:95], a[228:231], v[112:115], v[80:95]\n"
    "s_waitcnt lgkmcnt(1)\n"
    "v_mfma_f32_32x32x16_bf16 v[80:95], a[236:239], v[116:119], v[80:95]\n"
    "global_load_dwordx4 a[100:103], v61, s[16:17] offset:-1024\n"
    "v_mfma_f32_32x32x16_bf16 v[80:95], a[244:247], v[96:99], v[80:95]\n"
    "s_mov_b64 exec, 0xff\n"
    "s_waitcnt vmcnt(15)\n"
    "v_cmp_ne_u32 vcc, s98, v60\n"
    "s_mov_b64 exec, -1\n"
    "s_cbranch_vccz .Lpd3\n"
    "s_mov_b64 exec, 255\n"
    ".Lps3:\n"
    "s_sleep 1\n"
    "buffer_load_dword v60, v11, s[16:19], s82 offen sc1\n"
    "s_waitcnt vmcnt(0)\n"
    "v_cmp_ne_u32 vcc, s98, v60\n"
    "s_nop 1\n"
    "s_cbranch_vccz .Lpe3\n"
    "s_sub_u32 s67, s67, 1\n"
    "s_cbranch_scc0 .Lps3\n"
    "s_mov_b32 s67, 0\n"
    ".Lpe3:\n"
    "s_mov_b64 exec, -1\n"
    ".Lpd3:\n"
    "s_add_u32 m0, s49, 0x10000\n"
    "s_add_u32 s87, s31, 0x980000\n"
    "v_add_u32 v62, s87, v1\n"
    "global_load_lds_dwordx4 v62, s[16:17] sc1\n"
    "global_load_lds_dwordx4 v62, s[16:17] offset:1024 sc1\n"
    "s_waitcnt lgkmcnt(0)\n"
    "v_mfma_f32_32x32x16_bf16 v[80:95], a[252:255], v[100:103], v[80:95]\n"
    "v_mfma_f32_32x32x16_bf16 v[64:79], a[224:227], v[112:115], v[64:79]\n"
    "s_nop 3\n"
    "ds_write_b128 v5, v[80:83] offset:0\n"
    "v_mfma_f32_32x32x16_bf16 v[64:79], a[232:235], v[116:119], v[64:79]\n"
    "s_add_u32 m0, s49, 0x12000\n"
    "s_add_u32 s87, s31, 0x982000\n"
    "v_add_u32 v62, s87, v1\n"
    "global_load_lds_dwordx4 v62, s[16:17] sc1\n"
    "global_load_lds_dwordx4 v62, s[16:17] offset:1024 sc1\n"
    "ds_write_b128 v5, v[84:87] offset:1024\n"
    "v_mfma_f32_32x32x16_bf16 v[64:79], a[240:243], v[96:99], v[64:79]\n"
    "v_mfma_f32_32x32x16_bf16 v[64:79], a[248:251], v[100:103], v[64:79]\n"
    "ds_write_b128 v5, v[88:91] offset:2048\n"
    "ds_write_b128 v5, v[92:95] offset:3072\n"
    "s_nop 7\n"
    "s_nop 1\n"
    "ds_write_b128 v5, v[72:75] offset:4096\n"
    "ds_write_b128 v5, v[76:79] offset:5120\n"
    "s_add_u32 m0, s49, 0x18000\n"
    "s_add_u32 s87, s35, 0xb80000\n"
    "v_add_u32 v62, s87, v1\n"
    "global_load_lds_dwordx4 v62, s[16:17] sc1\n"
    "global_load_lds_dwordx4 v62, s[16:17] offset:1024 sc1\n"
    "v_add_f32 v24, v24, v64\n"
    "v_add_f32 v25, v25, v65\n"
    "v_add_f32 v26, v26, v66\n"
    "v_add_f32 v27, v27, v67\n"
    "v_add_f32 v28, v28, v68\n"
    "v_add_f32 v29, v29, v69\n"
    "v_add_f32 v30, v30, v70\n"
    "v_add_f32 v31, v31, v71\n"
    "s_waitcnt lgkmcnt(0)\n"
    "s_barrier\n"
    "s_add_u32 m0, s50, 0x14000\n"
    "s_add_u32 s87, s96, 0x980000\n"
    "v_add_u32 v62, s87, v1\n"
    "global_load_lds_dwordx4 v62, s[16:17] sc1\n"
    "global_load_lds_dwordx4 v62, s[16:17] offset:1024 sc1\n"
    "global_load_lds_dwordx4 v62, s[16:17] offset:2048 sc1\n"
    "global_load_lds_dwordx4 v62, s[16:17] offset:3072 sc1\n"
    "ds_read_b128 v[104:107], v121 offset:0\n"
    "ds_read_b128 v[108:111], v121 offset:1024\n"
    "ds_read_b128 v[112:115], v122 offset:0\n"
    "ds_read_b128 v[116:119], v122 offset:1024\n"
    "ds_read_b128 v[96:99], v120 offset:0\n"
    "ds_read_b128 v[100:103], v120 offset:1024\n"
    "s_waitcnt lgkmcnt(2)\n"
    "v_add_f32 v104, v104, v112\n"
    "v_add_f32 v105, v105, v113\n"
    "v_add_f32 v106, v106, v114\n"
    "v_add_f32 v107, v107, v115\n"
    "v_add_f32 v108, v108, v116\n"
    "v_add_f32 v109, v109, v117\n"
    "v_add_f32 v110, v110, v118\n"
    "v_add_f32 v111, v111, v119\n"
    "s_waitcnt lgkmcnt(0)\n"
    "v_add_f32 v24, v24, v96\n"
    "v_add_f32 v25, v25, v97\n"
    "v_add_f32 v26, v26, v98\n"
    "v_add_f32 v27, v27, v99\n"
    "v_add_f32 v28, v28, v100\n"
    "v_add_f32 v29, v29, v101\n"
    "v_add_f32 v30, v30, v102\n"
    "v_add_f32 v31, v31, v103\n"
    "v_add_f32 v24, v24, v104\n"
    "v_add_f32 v25, v25, v105\n"
    "v_add_f32 v26, v26, v106\n"
    "v_add_f32 v27, v27, v107\n"
    "v_add_f32 v28, v28, v108\n"
    "v_add_f32 v29, v29, v109\n"
    "v_add_f32 v30, v30, v110\n"
    "v_add_f32 v31, v31, v111\n"
    "v_cvt_pk_bf16_f32 v124, v24, v25\n"
    "v_cvt_pk_bf16_f32 v125, v26, v27\n"
    "v_cvt_pk_bf16_f32 v126, v28, v29\n"
    "v_cvt_pk_bf16_f32 v127, v30, v31\n"
    "s_add_u32 s84, s42, 0x988000\n"
    "buffer_store_dwordx4 v[124:127], v8, s[16:19], s84 offen sc1\n"
    "v_add_f32 v128, v24, v25\n"
    "v_mul_f32 v129, v24, v24\n"
    "v_add_f32 v130, v26, v27\n"
    "v_mul_f32 v131, v25, v25\n"
    "v_fma_f32 v129, v26, v26, v129\n"
    "v_fma_f32 v131, v27, v27, v131\n"
    "v_add_f32 v128, v128, v28\n"
    "v_add_f32 v130, v130, v29\n"
    "v_fma_f32 v129, v28, v28, v129\n"
    "v_fma_f32 v131, v29, v29, v131\n"
    "v_add_f32 v128, v128, v30\n"
    "v_add_f32 v130, v130, v31\n"
    "v_fma_f32 v129, v30, v30, v129\n"
    "v_fma_f32 v131, v31, v31, v131\n"
    "v_add_f32 v128, v128, v130\n"
    "v_add_f32 v129, v129, v131\n"
    "s_add_u32 s85, s43, 0xb82000\n"
    "global_load_dwordx4 a[104:107], v61, s[16:17]\n"
    "v_permlane32_swap_b32 v128, v129\n"
    "v_add_f32 v128, v128, v129\n"
    "buffer_store_dword v128, v9, s[16:19], s85 offen sc1\n"
    "global_load_dwordx4 a[108:111], v61, s[16:17] offset:1024\n"
    "global_load_dwordx4 a[112:115], v61, s[16:17] offset:2048\n"
    "global_load_dwordx4 a[116:119], v61, s[16:17] offset:3072\n"
    "s_waitcnt vmcnt(14)\n"
    "s_barrier\n"
    "ds_read_b128 v[96:99], v3 offset:16384\n"
    "ds_read_b128 v[100:103], v3 offset:17408\n"
    "ds_read_b128 v[104:107], v3 offset:18432\n"
    "ds_read_b128 v[108:111], v3 offset:19456\n"
    "v_add_u32 v61, 0x2000, v61\n"
    "global_load_dwordx4 a[120:123], v61, s[16:17] offset:-4096\n"
    "global_load_dwordx4 a[124:127], v61, s[16:17] offset:-3072\n"
    "s_waitcnt lgkmcnt(2)\n"
    "v_mfma_f32_16x16x32_bf16 v[64:67], v[152:155], v[96:99], 0\n"
    "v_mfma_f32_16x16x32_bf16 v[68:71], v[156:159], v[96:99], 0\n"
    "v_mfma_f32_16x16x32_bf16 v[72:75], v[160:163], v[96:99], 0\n"
    "ds_read_b128 v[96:99], v3 offset:20480\n"
    "global_load_dwordx4 a[128:131], v61, s[16:17] offset:-2048\n"
    "v_mfma_f32_16x16x32_bf16 v[64:67], v[164:167], v[100:103], v[64:67]\n"
    "v_mfma_f32_16x16x32_bf16 v[68:71], v[168:171], v[100:103], v[68:71]\n"
    "v_mfma_f32_16x16x32_bf16 v[72:75], v[172:175], v[100:103], v[72:75]\n"
    "ds_read_b128 v[100:103], v3 offset:21504\n"
    "s_waitcnt lgkmcnt(2)\n"
    "v_mfma_f32_16x16x32_bf16 v[64:67], v[176:179], v[104:107], v[64:67]\n"
    "global_load_dwordx4 a[132:135], v61, s[16:17] offset:-1024\n"
    "v_mfma_f32_16x16x32_bf16 v[68:71], v[180:183], v[104:107], v[68:71]\n"
    "v_mfma_f32_16x16x32_bf16 v[72:75], v[184:187], v[104:107], v[72:75]\n"
    "ds_read_b128 v[104:107], v3 offset:22528\n"
    "v_mfma_f32_16x16x32_bf16 v[64:67], v[188:191], v[108:111], v[64:67]\n"
    "global_load_dwordx4 a[136:139], v61, s[16:17]\n"
    "v_mfma_f32_16x16x32_bf16 v[68:71], v[192:195], v[108:111], v[68:71]\n"
    "v_mfma_f32_16x16x32_bf16 v[72:75], v[196:199], v[108:111], v[72:75]\n"
    "s_waitcnt vmcnt(15)\n"
    "s_barrier\n"
    "s_waitcnt lgkmcnt(2)\n"
    "v_mfma_f32_16x16x32_bf16 v[64:67], v[200:203], v[96:99], v[64:67]\n"
    "ds_read_b128 v[108:111], v3 offset:23552\n"
    "s_add_u32 s63, s71, 0x18000\n"
    "v_add_u32 v147, s63, v12\n"
    "ds_read_b64 v[128:129], v147 offset:0\n"
    "v_mfma_f32_16x16x32_bf16 v[68:71], v[204:207], v[96:99], v[68:71]\n"
    "ds_read_b64 v[130:131], v147 offset:512\n"
    "ds_read_b64 v[132:133], v147 offset:1024\n"
    "v_mfma_f32_16x16x32_bf16 v[72:75], v[208:211], v[96:99], v[72:75]\n"
    "ds_read_b128 v[96:99], v3 offset:24576\n"
    "ds_read_b64 v[134:135], v147 offset:1536\n"
    "ds_read_b64 v[136:137], v147 offset:2048\n"
    "s_waitcnt lgkmcnt(7)\n"
    "v_mfma_f32_16x16x32_bf16 v[64:67], v[212:215], v[100:103], v[64:67]\n"
    "ds_read_b64 v[138:139], v147 offset:2560\n"
    "ds_read_b64 v[140:141], v147 offset:3072\n"
    "v_mfma_f32_16x16x32_bf16 v[68:71], v[216:219], v[100:103], v[68:71]\n"
    "ds_read_b64 v[142:143], v147 offset:3584\n"
    "global_load_dwordx4 a[140:143], v61, s[16:17] offset:1024\n"
    "v_mfma_f32_16x16x32_bf16 v[72:75], v[220:223], v[100:103], v[72:75]\n"
    "ds_read_b128 v[100:103], v3 offset:25600\n"
    "global_load_dwordx4 a[144:147], v61, s[16:17] offset:2048\n"
    "v_mfma_f32_16x16x32_bf16 v[64:67], v[224:227], v[104:107], v[64:67]\n"
    "v_mfma_f32_16x16x32_bf16 v[68:71], v[228:231], v[104:107], v[68:71]\n"
    "global_load_dwordx4 a[148:151], v61, s[16:17] offset:3072\n"
    "v_mfma_f32_16x16x32_bf16 v[72:75], v[232:235], v[104:107], v[72:75]\n"
    "ds_read_b128 v[104:107], v3 offset:26624\n"
    "s_waitcnt lgkmcnt(6)\n"
    "v_mfma_f32_16x16x32_bf16 v[64:67], v[236:239], v[108:111], v[64:67]\n"
    "s_waitcnt lgkmcnt(5)\n"
    "v_add_f32 v128, v128, v136\n"
    "v_add_f32 v129, v129, v137\n"
    "s_waitcnt lgkmcnt(3)\n"
    "v_add_f32 v130, v130, v138\n"
    "v_add_f32 v131, v131, v139\n"
    "v_add_f32 v132, v132, v140\n"
    "v_add_f32 v133, v133, v141\n"
    "s_waitcnt lgkmcnt(2)\n"
    "v_add_f32 v134, v134, v142\n"
    "v_add_f32 v135, v135, v143\n"
    "v_add_f32 v128, v128, v132\n"
    "v_add_f32 v129, v129, v133\n"
    "v_add_f32 v130, v130, v134\n"
    "v_add_f32 v131, v131, v135\n"
    "v_mfma_f32_16x16x32_bf16 v[68:71], v[240:243], v[108:111], v[68:71]\n"
    "v_add_f32 v128, v128, v130\n"
    "v_add_f32 v129, v129, v131\n"
    "v_mfma_f32_16x16x32_bf16 v[72:75], v[244:247], v[108:111], v[72:75]\n"
    "ds_read_b128 v[108:111], v3 offset:27648\n"
    "v_permlane32_swap_b32 v128, v129\n"
    "v_mfma_f32_16x16x32_bf16 v[64:67], v[248:251], v[96:99], v[64:67]\n"
    "v_add_f32 v128, v128, v129\n"
    "v_mov_b32 v130, v128\n"
    "v_mfma_f32_16x16x32_bf16 v[68:71], v[252:255], v[96:99], v[68:71]\n"
    "v_permlane16_swap_b32 v128, v130\n"
    "v_mfma_f32_16x16x32_bf16 v[72:75], a[0:3], v[96:99], v[72:75]\n"
    "ds_read_b128 v[96:99], v3 offset:28672\n"
    "v_add_f32 v128, v128, v130\n"
    "v_mov_b32 v129, v128\n"
    "s_waitcnt lgkmcnt(2)\n"
    "v_mfma_f32_16x16x32_bf16 v[64:67], a[4:7], v[100:103], v[64:67]\n"
    "v_permlane32_swap_b32 v128, v129\n"
    "v_mul_f32 v131, s79, v128\n"
    "v_mfma_f32_16x16x32_bf16 v[68:71], a[8:11], v[100:103], v[68:71]\n"
    "v_mov_b32 v132, s81\n"
    "v_fma_f32 v132, v129, s80, v132\n"
    "v_mfma_f32_16x16x32_bf16 v[72:75], a[12:15], v[100:103], v[72:75]\n"
    "ds_read_b128 v[100:103], v3 offset:29696\n"
    "v_fma_f32 v132, -v131, v131, v132\n"
    "v_max_f32 v132, s81, v132\n"
    "v_mfma_f32_16x16x32_bf16 v[64:67], a[16:19], v[104:107], v[64:67]\n"
    "s_waitcnt vmcnt(11)\n"
    "s_barrier\n"
    "v_mov_b32 v62, s98\n"
    "s_add_u32 s83, s47, 0x1f12080\n"
    "s_mov_b64 exec, s[68:69]\n"
    "buffer_store_dword v62, off, s[16:19], s83 sc1\n"
    "s_mov_b64 exec, -1\n"
    "v_rsq_f32 v133, v132\n"
    "v_mfma_f32_16x16x32_bf16 v[68:71], a[20:23], v[104:107], v[68:71]\n"
    "v_cvt_pk_bf16_f32 v136, v131, v131\n"
    "v_lshlrev_b32 v134, 16, v136\n"
    "v_mfma_f32_16x16x32_bf16 v[72:75], a[24:27], v[104:107], v[72:75]\n"
    "ds_read_b128 v[104:107], v3 offset:30720\n"
    "v_sub_f32 v134, v131, v134\n"
    "v_cvt_pk_bf16_f32 v137, v134, 0\n"
    "s_waitcnt lgkmcnt(2)\n"
    "v_mfma_f32_16x16x32_bf16 v[64:67], a[28:31], v[108:111], v[64:67]\n"
    "v_mul_f32 v135, v132, v133\n"
    "v_cvt_pk_bf16_f32 v138, v135, 0\n"
    "v_lshlrev_b32 v134, 16, v138\n"
    "v_mfma_f32_16x16x32_bf16 v[68:71], a[32:35], v[108:111], v[68:71]\n"
    "v_sub_f32 v134, v135, v134\n"
    "v_cvt_pk_bf16_f32 v139, v134, 0\n"
    "v_mfma_f32_16x16x32_bf16 v[72:75], a[36:39], v[108:111], v[72:75]\n"
    "ds_read_b128 v[108:111], v3 offset:31744\n"
    "v_mul_f32 v133, s78, v133\n"
    "v_add_u32 v147, s73, v150\n"
    "v_mfma_f32_16x16x32_bf16 v[64:67], a[40:43], v[96:99], v[64:67]\n"
    "ds_write_b128 v147, v[136:139] offset:0\n"
    "v_mfma_f32_16x16x32_bf16 v[68:71], a[44:47], v[96:99], v[68:71]\n"
    "v_add_u32 v147, s75, v14\n"
    "v_add_u32 v61, 0x2000, v61\n"
    "global_load_dwordx4 a[152:155], v61, s[16:17] offset:-4096\n"
    "v_mfma_f32_16x16x32_bf16 v[72:75], a[48:51], v[96:99], v[72:75]\n"
    "ds_write_b32 v147, v133 offset:1536\n"
    "s_waitcnt lgkmcnt(0)\n"
    "s_barrier\n"
    "v_mfma_f32_16x16x32_bf16 v[64:67], a[52:55], v[100:103], v[64:67]\n"
    "ds_read_b64 v[122:123], v15 offset:1024\n"
    "ds_read_b64 v[120:121], v15 offset:512\n"
    "v_mfma_f32_16x16x32_bf16 v[68:71], a[56:59], v[100:103], v[68:71]\n"
    "ds_read_b128 v[112:115], v3 offset:32768\n"
    "ds_read_b128 v[116:119], v3 offset:33792\n"
    "v_mfma_f32_16x16x32_bf16 v[72:75], a[60:63], v[100:103], v[72:75]\n"
    "ds_read_b32 v144, v14 offset:1600\n"
    "ds_read_b32 v145, v14 offset:1664\n"
    "v_mfma_f32_16x16x32_bf16 v[64:67], a[64:67], v[104:107], v[64:67]\n"
    "ds_read_b128 v[96:99], v3 offset:34816\n"
    "ds_read_b128 v[100:103], v3 offset:35840\n"
    "v_mfma_f32_16x16x32_bf16 v[68:71], a[68:71], v[104:107], v[68:71]\n"
    "global_load_dwordx4 a[156:159], v61, s[16:17] offset:-3072\n"
    "v_mfma_f32_16x16x32_bf16 v[72:75], a[72:75], v[104:107], v[72:75]\n"
    "ds_read_b128 v[104:107], v3 offset:36864\n"
    "global_load_dwordx4 a[160:163], v61, s[16:17] offset:-2048\n"
    "v_mfma_f32_16x16x32_bf16 v[64:67], a[76:79], v[108:111], v[64:67]\n"
    "global_load_dwordx4 a[164:167], v61, s[16:17] offset:-1024\n"
    "v_mfma_f32_16x16x32_bf16 v[68:71], a[80:83], v[108:111], v[68:71]\n"
    "v_mfma_f32_16x16x32_bf16 v[72:75], a[84:87], v[108:111], v[72:75]\n"
    "ds_read_b128 v[108:111], v3 offset:37888\n"
    "s_waitcnt lgkmcnt(6)\n"
    "v_mfma_f32_16x16x16_bf16 v[76:79], v[54:55], v[122:123], 0\n"
    "global_load_dwordx4 a[168:171], v61, s[16:17]\n"
    "v_mfma_f32_16x16x16_bf16 v[80:83], v[56:57], v[122:123], 0\n"
    "v_mfma_f32_16x16x16_bf16 v[84:87], v[58:59], v[122:123], 0\n"
    "v_mfma_f32_16x16x16_bf16 v[64:67], v[54:55], v[120:121], v[64:67]\n"
    "global_load_dwordx4 a[172:175], v61, s[16:17] offset:1024\n"
    "v_mfma_f32_16x16x16_bf16 v[68:71], v[56:57], v[120:121], v[68:71]\n"
    "v_mfma_f32_16x16x16_bf16 v[72:75], v[58:59], v[120:121], v[72:75]\n"
    "s_waitcnt lgkmcnt(1)\n"
    "v_mul_f32 v64, v64, v144\n"
    "v_mfma_f32_16x16x32_bf16 v[76:79], v[152:155], v[112:115], v[76:79]\n"
    "v_mul_f32 v65, v65, v144\n"
    "v_mul_f32 v66, v66, v144\n"
    "v_mfma_f32_16x16x32_bf16 v[80:83], v[156:159], v[112:115], v[80:83]\n"
    "v_mul_f32 v67, v67, v144\n"
    "v_exp_f32 v132, v64\n"
    "v_mfma_f32_16x16x32_bf16 v[84:87], v[160:163], v[112:115], v[84:87]\n"
    "ds_read_b128 v[112:115], v3 offset:38912\n"
    "v_exp_f32 v133, v65\n"
    "v_mfma_f32_16x16x32_bf16 v[76:79], v[164:167], v[116:119], v[76:79]\n"
    "v_exp_f32 v134, v66\n"
    "v_mfma_f32_16x16x32_bf16 v[80:83], v[168:171], v[116:119], v[80:83]\n"
    "v_exp_f32 v135, v67\n"
    "v_mfma_f32_16x16x32_bf16 v[84:87], v[172:175], v[116:119], v[84:87]\n"
    "ds_read_b128 v[116:119], v3 offset:39936\n"
    "v_fma_f32 v136, v132, 0.5, 1.0\n"
    "v_mfma_f32_16x16x32_bf16 v[76:79], v[176:179], v[96:99], v[76:79]\n"
    "v_fma_f32 v137, v133, 0.5, 1.0\n"
    "v_fma_f32 v138, v134, 0.5, 1.0\n"
    "v_mfma_f32_16x16x32_bf16 v[80:83], v[180:183], v[96:99], v[80:83]\n"
    "v_fma_f32 v139, v135, 0.5, 1.0\n"
    "v_fma_f32 v136, v132, v136, 1.0\n"
    "v_mfma_f32_16x16x32_bf16 v[84:87], v[184:187], v[96:99], v[84:87]\n"
    "ds_read_b128 v[96:99], v3 offset:40960\n"
    "v_fma_f32 v137, v133, v137, 1.0\n"
    "v_fma_f32 v138, v134, v138, 1.0\n"
    "v_fma_f32 v139, v135, v139, 1.0\n"
    "v_mfma_f32_16x16x32_bf16 v[76:79], v[188:191], v[100:103], v[76:79]\n"
    "v_rcp_f32 v136, v136\n"
    "v_mfma_f32_16x16x32_bf16 v[80:83], v[192:195], v[100:103], v[80:83]\n"
    "v_rcp_f32 v137, v137\n"
    "v_mfma_f32_16x16x32_bf16 v[84:87], v[196:199], v[100:103], v[84:87]\n"
    "ds_read_b128 v[100:103], v3 offset:41984\n"
    "v_rcp_f32 v138, v138\n"
    "v_mfma_f32_16x16x32_bf16 v[76:79], v[200:203], v[104:107], v[76:79]\n"
    "s_mov_b32 s82, 0x1f12080\n"
    "s_mov_b64 exec, 0xff\n"
    "buffer_load_dword v60, v11, s[16:19], s82 offen sc1\n"
    "s_mov_b64 exec, -1\n"
    "v_rcp_f32 v139, v139\n"
    "v_mfma_f32_16x16x32_bf16 v[80:83], v[204:207], v[104:107], v[80:83]\n"
    "v_fma_f32 v64, -v64, v136, v64\n"
    "v_fma_f32 v65, -v65, v137, v65\n"
    "v_mfma_f32_16x16x32_bf16 v[84:87], v[208:211], v[104:107], v[84:87]\n"
    "ds_read_b128 v[104:107], v3 offset:43008\n"
    "v_fma_f32 v66, -v66, v138, v66\n"
    "v_fma_f32 v67, -v67, v139, v67\n"
    "s_waitcnt lgkmcnt(2)\n"
    "v_mfma_f32_16x16x32_bf16 v[76:79], v[212:215], v[108:111], v[76:79]\n"
    "v_cvt_pk_bf16_f32 v120, v64, v65\n"
    "v_cvt_pk_bf16_f32 v121, v66, v67\n"
    "v_mfma_f32_16x16x32_bf16 v[80:83], v[216:219], v[108:111], v[80:83]\n"
    "v_mul_f32 v68, v68, v144\n"
    "v_mul_f32 v69, v69, v144\n"
    "v_mul_f32 v70, v70, v144\n"
    "v_mfma_f32_16x16x32_bf16 v[84:87], v[220:223], v[108:111], v[84:87]\n"
    "ds_read_b128 v[108:111], v3 offset:44032\n"
    "v_mul_f32 v71, v71, v144\n"
    "v_exp_f32 v132, v68\n"
    "v_mfma_f32_16x16x32_bf16 v[76:79], v[224:227], v[112:115], v[76:79]\n"
    "v_exp_f32 v133, v69\n"
    "v_mfma_f32_16x16x32_bf16 v[80:83], v[228:231], v[112:115], v[80:83]\n"
    "v_exp_f32 v134, v70\n"
    "v_mfma_f32_16x16x32_bf16 v[84:87], v[232:235], v[112:115], v[84:87]\n"
    "ds_read_b128 v[112:115], v3 offset:45056\n"
    "v_exp_f32 v135, v71\n"
    "v_mfma_f32_16x16x32_bf16 v[76:79], v[236:239], v[116:119], v[76:79]\n"
    "v_fma_f32 v136, v132, 0.5, 1.0\n"
    "v_fma_f32 v137, v133, 0.5, 1.0\n"
    "v_mfma_f32_16x16x32_bf16 v[80:83], v[240:243], v[116:119], v[80:83]\n"
    "v_fma_f32 v138, v134, 0.5, 1.0\n"
    "v_fma_f32 v139, v135, 0.5, 1.0\n"
    "v_mfma_f32_16x16x32_bf16 v[84:87], v[244:247], v[116:119], v[84:87]\n"
    "ds_read_b128 v[116:119], v3 offset:46080\n"
    "v_fma_f32 v136, v132, v136, 1.0\n"
    "v_mfma_f32_16x16x32_bf16 v[76:79], v[248:251], v[96:99], v[76:79]\n"
    "v_fma_f32 v137, v133, v137, 1.0\n"
    "v_fma_f32 v138, v134, v138, 1.0\n"
    "v_fma_f32 v139, v135, v139, 1.0\n"
    "v_mfma_f32_16x16x32_bf16 v[80:83], v[252:255], v[96:99], v[80:83]\n"
    "v_rcp_f32 v136, v136\n"
    "v_mfma_f32_16x16x32_bf16 v[84:87], a[0:3], v[96:99], v[84:87]\n"
    "ds_read_b128 v[96:99], v3 offset:47104\n"
    "v_rcp_f32 v137, v137\n"
    "s_waitcnt lgkmcnt(2)\n"
    "v_mfma_f32_16x16x32_bf16 v[76:79], a[4:7], v[100:103], v[76:79]\n"
    "v_rcp_f32 v138, v138\n"
    "v_mfma_f32_16x16x32_bf16 v[80:83], a[8:11], v[100:103], v[80:83]\n"
    "v_rcp_f32 v139, v139\n"
    "v_mfma_f32_16x16x32_bf16 v[84:87], a[12:15], v[100:103], v[84:87]\n"
    "ds_read_b128 v[100:103], v3 offset:48128\n"
    "v_fma_f32 v68, -v68, v136, v68\n"
    "v_fma_f32 v69, -v69, v137, v69\n"
    "v_mfma_f32_16x16x32_bf16 v[76:79], a[16:19], v[104:107], v[76:79]\n"
    "v_fma_f32 v70, -v70, v138, v70\n"
    "v_fma_f32 v71, -v71, v139, v71\n"
    "v_mfma_f32_16x16x32_bf16 v[80:83], a[20:23], v[104:107], v[80:83]\n"
    "v_cvt_pk_bf16_f32 v124, v68, v69\n"
    "v_cvt_pk_bf16_f32 v125, v70, v71\n"
    "v_mfma_f32_16x16x32_bf16 v[84:87], a[24:27], v[104:107], v[84:87]\n"
    "v_mul_f32 v72, v72, v144\n"
    "v_mul_f32 v73, v73, v144\n"
    "v_mul_f32 v74, v74, v144\n"
    "v_mfma_f32_16x16x32_bf16 v[76:79], a[28:31], v[108:111], v[76:79]\n"
    "v_mul_f32 v75, v75, v144\n"
    "v_exp_f32 v132, v72\n"
    "v_mfma_f32_16x16x32_bf16 v[80:83], a[32:35], v[108:111], v[80:83]\n"
    "v_exp_f32 v133, v73\n"
    "v_mfma_f32_16x16x32_bf16 v[84:87], a[36:39], v[108:111], v[84:87]\n"
    "v_exp_f32 v134, v74\n"
    "v_mfma_f32_16x16x32_bf16 v[76:79], a[40:43], v[112:115], v[76:79]\n"
    "v_exp_f32 v135, v75\n"
    "v_mfma_f32_16x16x32_bf16 v[80:83], a[44:47], v[112:115], v[80:83]\n"
    "v_fma_f32 v136, v132, 0.5, 1.0\n"
    "v_fma_f32 v137, v133, 0.5, 1.0\n"
    "v_mfma_f32_16x16x32_bf16 v[84:87], a[48:51], v[112:115], v[84:87]\n"
    "v_fma_f32 v138, v134, 0.5, 1.0\n"
    "s_waitcnt lgkmcnt(0)\n"
    "v_mfma_f32_16x16x32_bf16 v[76:79], a[52:55], v[116:119], v[76:79]\n"
    "v_fma_f32 v139, v135, 0.5, 1.0\n"
    "v_fma_f32 v136, v132, v136, 1.0\n"
    "v_mfma_f32_16x16x32_bf16 v[80:83], a[56:59], v[116:119], v[80:83]\n"
    "v_fma_f32 v137, v133, v137, 1.0\n"
    "v_fma_f32 v138, v134, v138, 1.0\n"
    "v_fma_f32 v139, v135, v139, 1.0\n"
    "v_mfma_f32_16x16x32_bf16 v[84:87], a[60:63], v[116:119], v[84:87]\n"
    "v_rcp_f32 v136, v136\n"
    "v_mfma_f32_16x16x32_bf16 v[76:79], a[64:67], v[96:99], v[76:79]\n"
    "v_rcp_f32 v137, v137\n"
    "v_mfma_f32_16x16x32_bf16 v[80:83], a[68:71], v[96:99], v[80:83]\n"
    "v_rcp_f32 v138, v138\n"
    "v_mfma_f32_16x16x32_bf16 v[84:87], a[72:75], v[96:99], v[84:87]\n"
    "v_rcp_f32 v139, v139\n"
    "v_mfma_f32_16x16x32_bf16 v[76:79], a[76:79], v[100:103], v[76:79]\n"
    "v_fma_f32 v72, -v72, v136, v72\n"
    "v_fma_f32 v73, -v73, v137, v73\n"
    "v_mfma_f32_16x16x32_bf16 v[80:83], a[80:83], v[100:103], v[80:83]\n"
    "v_fma_f32 v74, -v74, v138, v74\n"
    "v_fma_f32 v75, -v75, v139, v75\n"
    "v_mfma_f32_16x16x32_bf16 v[84:87], a[84:87], v[100:103], v[84:87]\n"
    "v_cvt_pk_bf16_f32 v128, v72, v73\n"
    "v_cvt_pk_bf16_f32 v129, v74, v75\n"
    "v_mul_f32 v76, v76, v145\n"
    "v_mul_f32 v77, v77, v145\n"
    "v_mul_f32 v78, v78, v145\n"
    "v_mul_f32 v79, v79, v145\n"
    "v_exp_f32 v132, v76\n"
    "v_exp_f32 v133, v77\n"
    "v_exp_f32 v134, v78\n"
    "v_exp_f32 v135, v79\n"
    "v_fma_f32 v136, v132, 0.5, 1.0\n"
    "v_fma_f32 v137, v133, 0.5, 1.0\n"
    "v_fma_f32 v138, v134, 0.5, 1.0\n"
    "v_fma_f32 v139, v135, 0.5, 1.0\n"
    "v_fma_f32 v136, v132, v136, 1.0\n"
    "v_fma_f32 v137, v133, v137, 1.0\n"
    "v_fma_f32 v138, v134, v138, 1.0\n"
    "v_fma_f32 v139, v135, v139, 1.0\n"
    "v_rcp_f32 v136, v136\n"
    "v_rcp_f32 v137, v137\n"
    "v_rcp_f32 v138, v138\n"
    "v_rcp_f32 v139, v139\n"
    "v_fma_f32 v76, -v76, v136, v76\n"
    "v_fma_f32 v77, -v77, v137, v77\n"
    "v_fma_f32 v78, -v78, v138, v78\n"
    "v_fma_f32 v79, -v79, v139, v79\n"
    "v_cvt_pk_bf16_f32 v122, v76, v77\n"
    "v_cvt_pk_bf16_f32 v123, v78, v79\n"
    "global_load_dwordx4 a[176:179], v61, s[16:17] offset:2048\n"
    "v_permlane16_swap_b32 v120, v122\n"
    "v_permlane16_swap_b32 v121, v123\n"
    "s_add_u32 s84, s45, 0xc00000\n"
    "buffer_store_dwordx4 v[120:123], v1, s[16:19], s84 offen sc1\n"
    "v_mul_f32 v80, v80, v145\n"
    "v_mul_f32 v81, v81, v145\n"
    "v_mul_f32 v82, v82, v145\n"
    "v_mul_f32 v83, v83, v145\n"
    "v_exp_f32 v132, v80\n"
    "v_exp_f32 v133, v81\n"
    "v_exp_f32 v134, v82\n"
    "v_exp_f32 v135, v83\n"
    "v_fma_f32 v136, v132, 0.5, 1.0\n"
    "v_fma_f32 v137, v133, 0.5, 1.0\n"
    "v_fma_f32 v138, v134, 0.5, 1.0\n"
    "v_fma_f32 v139, v135, 0.5, 1.0\n"
    "v_fma_f32 v136, v132, v136, 1.0\n"
    "s_mov_b64 exec, 0xff\n"
    "s_waitcnt vmcnt(2)\n"
    "v_cmp_ne_u32 vcc, s98, v60\n"
    "s_mov_b64 exec, -1\n"
    "s_cbranch_vccz .Lpd4\n"
    "s_mov_b64 exec, 255\n"
    ".Lps4:\n"
    "s_sleep 1\n"
    "buffer_load_dword v60, v11, s[16:19], s82 offen sc1\n"
    "s_waitcnt vmcnt(0)\n"
    "v_cmp_ne_u32 vcc, s98, v60\n"
    "s_nop 1\n"
    "s_cbranch_vccz .Lpe4\n"
    "s_sub_u32 s67, s67, 1\n"
    "s_cbranch_scc0 .Lps4\n"
    "s_mov_b32 s67, 0\n"
    ".Lpe4:\n"
    "s_mov_b64 exec, -1\n"
    ".Lpd4:\n"
    "s_add_u32 m0, s49, 0x1d000\n"
    "s_add_u32 s87, s31, 0x988000\n"
    "v_add_u32 v62, s87, v1\n"
    "global_load_lds_dwordx4 v62, s[16:17] sc1\n"
    "global_load_lds_dwordx4 v62, s[16:17] offset:1024 sc1\n"
    "s_add_u32 m0, s49, 0x1f000\n"
    "s_add_u32 s87, s31, 0x98a000\n"
    "v_add_u32 v62, s87, v1\n"
    "global_load_lds_dwordx4 v62, s[16:17] sc1\n"
    "global_load_lds_dwordx4 v62, s[16:17] offset:1024 sc1\n"
    "v_fma_f32 v137, v133, v137, 1.0\n"
    "v_fma_f32 v138, v134, v138, 1.0\n"
    "v_fma_f32 v139, v135, v139, 1.0\n"
    "v_rcp_f32 v136, v136\n"
    "v_rcp_f32 v137, v137\n"
    "v_rcp_f32 v138, v138\n"
    "v_rcp_f32 v139, v139\n"
    "v_fma_f32 v80, -v80, v136, v80\n"
    "v_fma_f32 v81, -v81, v137, v81\n"
    "v_fma_f32 v82, -v82, v138, v82\n"
    "v_fma_f32 v83, -v83, v139, v83\n"
    "v_cvt_pk_bf16_f32 v126, v80, v81\n"
    "v_cvt_pk_bf16_f32 v127, v82, v83\n"
    "s_nop 0\n"
    "v_permlane16_swap_b32 v124, v126\n"
    "v_permlane16_swap_b32 v125, v127\n"
    "buffer_store_dwordx4 v[124:127], v1, s[16:19], s84 offen offset:1024 sc1\n"
    "v_mul_f32 v84, v84, v145\n"
    "v_mul_f32 v85, v85, v145\n"
    "v_mul_f32 v86, v86, v145\n"
    "v_mul_f32 v87, v87, v145\n"
    "v_exp_f32 v132, v84\n"
    "v_exp_f32 v133, v85\n"
    "v_exp_f32 v134, v86\n"
    "v_exp_f32 v135, v87\n"
    "v_fma_f32 v136, v132, 0.5, 1.0\n"
    "v_fma_f32 v137, v133, 0.5, 1.0\n"
    "v_fma_f32 v138, v134, 0.5, 1.0\n"
    "v_fma_f32 v139, v135, 0.5, 1.0\n"
    "s_add_u32 m0, s49, 0x25000\n"
    "s_add_u32 s87, s35, 0xb82000\n"
    "v_add_u32 v62, s87, v1\n"
    "global_load_lds_dwordx4 v62, s[16:17] sc1\n"
    "global_load_lds_dwordx4 v62, s[16:17] offset:1024 sc1\n"
    "v_fma_f32 v136, v132, v136, 1.0\n"
    "v_fma_f32 v137, v133, v137, 1.0\n"
    "v_fma_f32 v138, v134, v138, 1.0\n"
    "v_fma_f32 v139, v135, v139, 1.0\n"
    "v_rcp_f32 v136, v136\n"
    "v_rcp_f32 v137, v137\n"
    "v_rcp_f32 v138, v138\n"
    "v_rcp_f32 v139, v139\n"
    "v_fma_f32 v84, -v84, v136, v84\n"
    "v_fma_f32 v85, -v85, v137, v85\n"
    "v_fma_f32 v86, -v86, v138, v86\n"
    "v_fma_f32 v87, -v87, v139, v87\n"
    "v_cvt_pk_bf16_f32 v130, v84, v85\n"
    "v_cvt_pk_bf16_f32 v131, v86, v87\n"
    "s_nop 0\n"
    "v_permlane16_swap_b32 v128, v130\n"
    "v_permlane16_swap_b32 v129, v131\n"
    "buffer_store_dwordx4 v[128:131], v151, s[16:19], s84 offen offset:2048 sc1\n"
    "s_add_u32 m0, s50, 0x21000\n"
    "s_add_u32 s87, s96, 0x988000\n"
    "v_add_u32 v62, s87, v1\n"
    "global_load_lds_dwordx4 v62, s[16:17] sc1\n"
    "global_load_lds_dwordx4 v62, s[16:17] offset:1024 sc1\n"
    "global_load_lds_dwordx4 v62, s[16:17] offset:2048 sc1\n"
    "global_load_lds_dwordx4 v62, s[16:17] offset:3072 sc1\n"
    "global_load_dwordx4 a[180:183], v61, s[16:17] offset:3072\n"
    "v_add_u32 v61, 0x2000, v61\n"
    "global_load_dwordx4 a[184:187], v61, s[16:17] offset:-4096\n"
    "global_load_dwordx4 a[188:191], v61, s[16:17] offset:-3072\n"
    "global_load_dwordx4 a[192:195], v61, s[16:17] offset:-2048\n"
    "s_waitcnt vmcnt(14)\n"
    "s_barrier\n"
    "ds_read_b128 v[96:99], v4 offset:20480\n"
    "ds_read_b128 v[100:103], v4 offset:21504\n"
    "ds_read_b128 v[104:107], v4 offset:22528\n"
    "ds_read_b128 v[108:111], v4 offset:23552\n"
    "global_load_dwordx4 a[196:199], v61, s[16:17] offset:-1024\n"
    "global_load_dwordx4 a[200:203], v61, s[16:17]\n"
    "s_waitcnt lgkmcnt(2)\n"
    "v_mfma_f32_16x16x32_bf16 v[64:67], v[152:155], v[96:99], 0\n"
    "v_mfma_f32_16x16x32_bf16 v[68:71], v[156:159], v[96:99], 0\n"
    "v_mfma_f32_16x16x32_bf16 v[72:75], v[160:163], v[96:99], 0\n"
    "ds_read_b128 v[96:99], v4 offset:24576\n"
    "global_load_dwordx4 a[204:207], v61, s[16:17] offset:1024\n"
    "v_mfma_f32_16x16x32_bf16 v[64:67], v[164:167], v[100:103], v[64:67]\n"
    "v_mfma_f32_16x16x32_bf16 v[68:71], v[168:171], v[100:103], v[68:71]\n"
    "v_mfma_f32_16x16x32_bf16 v[72:75], v[172:175], v[100:103], v[72:75]\n"
    "ds_read_b128 v[100:103], v4 offset:25600\n"
    "s_waitcnt lgkmcnt(2)\n"
    "v_mfma_f32_16x16x32_bf16 v[64:67], v[176:179], v[104:107], v[64:67]\n"
    "global_load_dwordx4 a[208:211], v61, s[16:17] offset:2048\n"
    "v_mfma_f32_16x16x32_bf16 v[68:71], v[180:183], v[104:107], v[68:71]\n"
    "v_mfma_f32_16x16x32_bf16 v[72:75], v[184:187], v[104:107], v[72:75]\n"
    "ds_read_b128 v[104:107], v4 offset:26624\n"
    "v_mfma_f32_16x16x32_bf16 v[64:67], v[188:191], v[108:111], v[64:67]\n"
    "global_load_dwordx4 a[212:215], v61, s[16:17] offset:3072\n"
    "v_mfma_f32_16x16x32_bf16 v[68:71], v[192:195], v[108:111], v[68:71]\n"
    "v_mfma_f32_16x16x32_bf16 v[72:75], v[196:199], v[108:111], v[72:75]\n"
    "ds_read_b128 v[108:111], v4 offset:27648\n"
    "s_waitcnt vmcnt(13)\n"
    "s_barrier\n"
    "s_barrier\n"
    "v_mov_b32 v62, s98\n"
    "s_add_u32 s83, s47, 0x1f14000\n"
    "s_mov_b64 exec, s[68:69]\n"
    "buffer_store_dword v62, off, s[16:19], s83 sc1\n"
    "s_mov_b64 exec, -1\n"
    "s_waitcnt lgkmcnt(1)\n"
    "v_mfma_f32_16x16x32_bf16 v[64:67], v[200:203], v[96:99], v[64:67]\n"
    "s_add_u32 s63, s71, 0x25000\n"
    "v_add_u32 v147, s63, v12\n"
    "ds_read_b64 v[128:129], v147 offset:0\n"
    "v_mfma_f32_16x16x32_bf16 v[68:71], v[204:207], v[96:99], v[68:71]\n"
    "ds_read_b64 v[130:131], v147 offset:512\n"
    "ds_read_b64 v[132:133], v147 offset:1024\n"
    "v_mfma_f32_16x16x32_bf16 v[72:75], v[208:211], v[96:99], v[72:75]\n"
    "ds_read_b128 v[96:99], v4 offset:28672\n"
    "ds_read_b64 v[134:135], v147 offset:1536\n"
    "ds_read_b64 v[136:137], v147 offset:2048\n"
    "v_mfma_f32_16x16x32_bf16 v[64:67], v[212:215], v[100:103], v[64:67]\n"
    "ds_read_b64 v[138:139], v147 offset:2560\n"
    "ds_read_b64 v[140:141], v147 offset:3072\n"
    "v_mfma_f32_16x16x32_bf16 v[68:71], v[216:219], v[100:103], v[68:71]\n"
    "ds_read_b64 v[142:143], v147 offset:3584\n"
    "v_add_u32 v61, 0x2000, v61\n"
    "global_load_dwordx4 a[216:219], v61, s[16:17] offset:-4096\n"
    "v_mfma_f32_16x16x32_bf16 v[72:75], v[220:223], v[100:103], v[72:75]\n"
    "ds_read_b128 v[100:103], v4 offset:29696\n"
    "global_load_dwordx4 a[220:223], v61, s[16:17] offset:-3072\n"
    "v_mfma_f32_16x16x32_bf16 v[64:67], v[224:227], v[104:107], v[64:67]\n"
    "v_mfma_f32_16x16x32_bf16 v[68:71], v[228:231], v[104:107], v[68:71]\n"
    "global_load_dwordx4 a[228:231], v61, s[16:17] offset:-2048\n"
    "v_mfma_f32_16x16x32_bf16 v[72:75], v[232:235], v[104:107], v[72:75]\n"
    "ds_read_b128 v[104:107], v4 offset:30720\n"
    "s_waitcnt lgkmcnt(6)\n"
    "v_mfma_f32_16x16x32_bf16 v[64:67], v[236:239], v[108:111], v[64:67]\n"
    "s_waitcnt lgkmcnt(4)\n"
    "v_add_f32 v128, v128, v136\n"
    "v_add_f32 v129, v129, v137\n"
    "v_add_f32 v130, v130, v138\n"
    "v_add_f32 v131, v131, v139\n"
    "s_waitcnt lgkmcnt(2)\n"
    "v_add_f32 v132, v132, v140\n"
    "v_add_f32 v133, v133, v141\n"
    "v_add_f32 v134, v134, v142\n"
    "v_add_f32 v135, v135, v143\n"
    "v_add_f32 v128, v128, v132\n"
    "v_add_f32 v129, v129, v133\n"
    "v_add_f32 v130, v130, v134\n"
    "v_add_f32 v131, v131, v135\n"
    "v_mfma_f32_16x16x32_bf16 v[68:71], v[240:243], v[108:111], v[68:71]\n"
    "v_add_f32 v128, v128, v130\n"
    "v_add_f32 v129, v129, v131\n"
    "v_mfma_f32_16x16x32_bf16 v[72:75], v[244:247], v[108:111], v[72:75]\n"
    "ds_read_b128 v[108:111], v4 offset:31744\n"
    "v_permlane32_swap_b32 v128, v129\n"
    "v_mfma_f32_16x16x32_bf16 v[64:67], v[248:251], v[96:99], v[64:67]\n"
    "v_add_f32 v128, v128, v129\n"
    "v_mov_b32 v130, v128\n"
    "v_mfma_f32_16x16x32_bf16 v[68:71], v[252:255], v[96:99], v[68:71]\n"
    "v_permlane16_swap_b32 v128, v130\n"
    "v_mfma_f32_16x16x32_bf16 v[72:75], a[0:3], v[96:99], v[72:75]\n"
    "ds_read_b128 v[96:99], v4 offset:32768\n"
    "v_add_f32 v128, v128, v130\n"
    "v_mov_b32 v129, v128\n"
    "s_waitcnt lgkmcnt(2)\n"
    "v_mfma_f32_16x16x32_bf16 v[64:67], a[4:7], v[100:103], v[64:67]\n"
    "v_permlane32_swap_b32 v128, v129\n"
    "v_mul_f32 v131, s79, v128\n"
    "v_mfma_f32_16x16x32_bf16 v[68:71], a[8:11], v[100:103], v[68:71]\n"
    "v_mov_b32 v132, s81\n"
    "v_fma_f32 v132, v129, s80, v132\n"
    "v_mfma_f32_16x16x32_bf16 v[72:75], a[12:15], v[100:103], v[72:75]\n"
    "ds_read_b128 v[100:103], v4 offset:33792\n"
    "v_fma_f32 v132, -v131, v131, v132\n"
    "v_max_f32 v132, s81, v132\n"
    "v_mfma_f32_16x16x32_bf16 v[64:67], a[16:19], v[104:107], v[64:67]\n"
    "v_rsq_f32 v133, v132\n"
    "v_mfma_f32_16x16x32_bf16 v[68:71], a[20:23], v[104:107], v[68:71]\n"
    "v_cvt_pk_bf16_f32 v136, v131, v131\n"
    "v_lshlrev_b32 v134, 16, v136\n"
    "v_mfma_f32_16x16x32_bf16 v[72:75], a[24:27], v[104:107], v[72:75]\n"
    "ds_read_b128 v[104:107], v4 offset:34816\n"
    "v_sub_f32 v134, v131, v134\n"
    "v_cvt_pk_bf16_f32 v137, v134, 0\n"
    "s_waitcnt lgkmcnt(2)\n"
    "v_mfma_f32_16x16x32_bf16 v[64:67], a[28:31], v[108:111], v[64:67]\n"
    "v_mul_f32 v135, v132, v133\n"
    "v_cvt_pk_bf16_f32 v138, v135, 0\n"
    "v_lshlrev_b32 v134, 16, v138\n"
    "v_mfma_f32_16x16x32_bf16 v[68:71], a[32:35], v[108:111], v[68:71]\n"
    "v_sub_f32 v134, v135, v134\n"
    "v_cvt_pk_bf16_f32 v139, v134, 0\n"
    "v_mfma_f32_16x16x32_bf16 v[72:75], a[36:39], v[108:111], v[72:75]\n"
    "ds_read_b128 v[108:111], v4 offset:35840\n"
    "v_mul_f32 v133, s78, v133\n"
    "v_add_u32 v147, s73, v150\n"
    "v_mfma_f32_16x16x32_bf16 v[64:67], a[40:43], v[96:99], v[64:67]\n"
    "ds_write_b128 v147, v[136:139] offset:2048\n"
    "v_mfma_f32_16x16x32_bf16 v[68:71], a[44:47], v[96:99], v[68:71]\n"
    "v_add_u32 v147, s75, v14\n"
    "global_load_dwordx4 a[236:239], v61, s[16:17] offset:-1024\n"
    "v_mfma_f32_16x16x32_bf16 v[72:75], a[48:51], v[96:99], v[72:75]\n"
    "ds_write_b32 v147, v133 offset:3584\n"
    "s_waitcnt vmcnt(11)\n"
    "s_waitcnt lgkmcnt(0)\n"
    "s_barrier\n"
    "v_mfma_f32_16x16x32_bf16 v[64:67], a[52:55], v[100:103], v[64:67]\n"
    "ds_read_b64 v[122:123], v15 offset:3072\n"
    "ds_read_b64 v[120:121], v15 offset:2560\n"
    "v_mfma_f32_16x16x32_bf16 v[68:71], a[56:59], v[100:103], v[68:71]\n"
    "ds_read_b128 v[112:115], v4 offset:36864\n"
    "ds_read_b128 v[116:119], v4 offset:37888\n"
    "v_mfma_f32_16x16x32_bf16 v[72:75], a[60:63], v[100:103], v[72:75]\n"
    "ds_read_b32 v144, v14 offset:3648\n"
    "ds_read_b32 v145, v14 offset:3712\n"
    "v_mfma_f32_16x16x32_bf16 v[64:67], a[64:67], v[104:107], v[64:67]\n"
    "ds_read_b128 v[96:99], v4 offset:38912\n"
    "ds_read_b128 v[100:103], v4 offset:39936\n"
    "v_mfma_f32_16x16x32_bf16 v[68:71], a[68:71], v[104:107], v[68:71]\n"
    "s_mov_b32 s82, 0x1f14000\n"
    "s_mov_b64 exec, 0xff\n"
    "buffer_load_dword v60, v11, s[16:19], s82 offen sc1\n"
    "s_mov_b64 exec, -1\n"
    "v_mfma_f32_16x16x32_bf16 v[72:75], a[72:75], v[104:107], v[72:75]\n"
    "ds_read_b128 v[104:107], v4 offset:40960\n"
    "global_load_dwordx4 a[244:247], v61, s[16:17]\n"
    "v_mfma_f32_16x16x32_bf16 v[64:67], a[76:79], v[108:111], v[64:67]\n"
    "global_load_dwordx4 a[252:255], v61, s[16:17] offset:1024\n"
    "v_mfma_f32_16x16x32_bf16 v[68:71], a[80:83], v[108:111], v[68:71]\n"
    "global_load_dwordx4 a[224:227], v61, s[16:17] offset:2048\n"
    "v_mfma_f32_16x16x32_bf16 v[72:75], a[84:87], v[108:111], v[72:75]\n"
    "ds_read_b128 v[108:111], v4 offset:41984\n"
    "s_waitcnt lgkmcnt(4)\n"
    "v_mfma_f32_16x16x16_bf16 v[76:79], v[54:55], v[122:123], 0\n"
    "v_mfma_f32_16x16x16_bf16 v[80:83], v[56:57], v[122:123], 0\n"
    "v_mfma_f32_16x16x16_bf16 v[84:87], v[58:59], v[122:123], 0\n"
    "global_load_dwordx4 a[232:235], v61, s[16:17] offset:3072\n"
    "v_mfma_f32_16x16x16_bf16 v[64:67], v[54:55], v[120:121], v[64:67]\n"
    "v_mfma_f32_16x16x16_bf16 v[68:71], v[56:57], v[120:121], v[68:71]\n"
    "v_mfma_f32_16x16x16_bf16 v[72:75], v[58:59], v[120:121], v[72:75]\n"
    "v_mul_f32 v64, v64, v144\n"
    "v_mul_f32 v65, v65, v144\n"
    "v_mfma_f32_16x16x32_bf16 v[76:79], v[152:155], v[112:115], v[76:79]\n"
    "v_mul_f32 v66, v66, v144\n"
    "v_mul_f32 v67, v67, v144\n"
    "v_mfma_f32_16x16x32_bf16 v[80:83], v[156:159], v[112:115], v[80:83]\n"
    "v_exp_f32 v132, v64\n"
    "v_mfma_f32_16x16x32_bf16 v[84:87], v[160:163], v[112:115], v[84:87]\n"
    "ds_read_b128 v[112:115], v4 offset:43008\n"
    "v_exp_f32 v133, v65\n"
    "v_mfma_f32_16x16x32_bf16 v[76:79], v[164:167], v[116:119], v[76:79]\n"
    "v_exp_f32 v134, v66\n"
    "v_mfma_f32_16x16x32_bf16 v[80:83], v[168:171], v[116:119], v[80:83]\n"
    "v_exp_f32 v135, v67\n"
    "v_mfma_f32_16x16x32_bf16 v[84:87], v[172:175], v[116:119], v[84:87]\n"
    "ds_read_b128 v[116:119], v4 offset:44032\n"
    "v_fma_f32 v136, v132, 0.5, 1.0\n"
    "s_waitcnt lgkmcnt(2)\n"
    "v_mfma_f32_16x16x32_bf16 v[76:79], v[176:179], v[96:99], v[76:79]\n"
    "v_fma_f32 v137, v133, 0.5, 1.0\n"
    "v_fma_f32 v138, v134, 0.5, 1.0\n"
    "v_mfma_f32_16x16x32_bf16 v[80:83], v[180:183], v[96:99], v[80:83]\n"
    "v_fma_f32 v139, v135, 0.5, 1.0\n"
    "v_fma_f32 v136, v132, v136, 1.0\n"
    "v_mfma_f32_16x16x32_bf16 v[84:87], v[184:187], v[96:99], v[84:87]\n"
    "ds_read_b128 v[96:99], v4 offset:45056\n"
    "v_fma_f32 v137, v133, v137, 1.0\n"
    "v_fma_f32 v138, v134, v138, 1.0\n"
    "v_fma_f32 v139, v135, v139, 1.0\n"
    "v_mfma_f32_16x16x32_bf16 v[76:79], v[188:191], v[100:103], v[76:79]\n"
    "v_rcp_f32 v136, v136\n"
    "v_mfma_f32_16x16x32_bf16 v[80:83], v[192:195], v[100:103], v[80:83]\n"
    "v_rcp_f32 v137, v137\n"
    "v_mfma_f32_16x16x32_bf16 v[84:87], v[196:199], v[100:103], v[84:87]\n"
    "ds_read_b128 v[100:103], v4 offset:46080\n"
    "v_rcp_f32 v138, v138\n"
    "v_mfma_f32_16x16x32_bf16 v[76:79], v[200:203], v[104:107], v[76:79]\n"
    "v_rcp_f32 v139, v139\n"
    "v_mfma_f32_16x16x32_bf16 v[80:83], v[204:207], v[104:107], v[80:83]\n"
    "v_fma_f32 v64, -v64, v136, v64\n"
    "v_fma_f32 v65, -v65, v137, v65\n"
    "v_mfma_f32_16x16x32_bf16 v[84:87], v[208:211], v[104:107], v[84:87]\n"
    "ds_read_b128 v[104:107], v4 offset:47104\n"
    "v_fma_f32 v66, -v66, v138, v66\n"
    "v_fma_f32 v67, -v67, v139, v67\n"
    "v_mfma_f32_16x16x32_bf16 v[76:79], v[212:215], v[108:111], v[76:79]\n"
    "v_cvt_pk_bf16_f32 v120, v64, v65\n"
    "v_cvt_pk_bf16_f32 v121, v66, v67\n"
    "v_mfma_f32_16x16x32_bf16 v[80:83], v[216:219], v[108:111], v[80:83]\n"
    "v_mul_f32 v68, v68, v144\n"
    "v_mul_f32 v69, v69, v144\n"
    "v_mul_f32 v70, v70, v144\n"
    "v_mfma_f32_16x16x32_bf16 v[84:87], v[220:223], v[108:111], v[84:87]\n"
    "ds_read_b128 v[108:111], v4 offset:48128\n"
    "v_mul_f32 v71, v71, v144\n"
    "v_exp_f32 v132, v68\n"
    "s_waitcnt lgkmcnt(2)\n"
    "v_mfma_f32_16x16x32_bf16 v[76:79], v[224:227], v[112:115], v[76:79]\n"
    "v_exp_f32 v133, v69\n"
    "v_mfma_f32_16x16x32_bf16 v[80:83], v[228:231], v[112:115], v[80:83]\n"
    "v_exp_f32 v134, v70\n"
    "v_mfma_f32_16x16x32_bf16 v[84:87], v[232:235], v[112:115], v[84:87]\n"
    "ds_read_b128 v[112:115], v4 offset:49152\n"
    "v_exp_f32 v135, v71\n"
    "v_mfma_f32_16x16x32_bf16 v[76:79], v[236:239], v[116:119], v[76:79]\n"
    "v_fma_f32 v136, v132, 0.5, 1.0\n"
    "v_fma_f32 v137, v133, 0.5, 1.0\n"
    "v_mfma_f32_16x16x32_bf16 v[80:83], v[240:243], v[116:119], v[80:83]\n"
    "v_fma_f32 v138, v134, 0.5, 1.0\n"
    "v_fma_f32 v139, v135, 0.5, 1.0\n"
    "v_mfma_f32_16x16x32_bf16 v[84:87], v[244:247], v[116:119], v[84:87]\n"
    "ds_read_b128 v[116:119], v4 offset:50176\n"
    "v_fma_f32 v136, v132, v136, 1.0\n"
    "v_mfma_f32_16x16x32_bf16 v[76:79], v[248:251], v[96:99], v[76:79]\n"
    "v_fma_f32 v137, v133, v137, 1.0\n"
    "v_fma_f32 v138, v134, v138, 1.0\n"
    "v_fma_f32 v139, v135, v139, 1.0\n"
    "v_mfma_f32_16x16x32_bf16 v[80:83], v[252:255], v[96:99], v[80:83]\n"
    "v_rcp_f32 v136, v136\n"
    "v_mfma_f32_16x16x32_bf16 v[84:87], a[0:3], v[96:99], v[84:87]\n"
    "ds_read_b128 v[96:99], v4 offset:51200\n"
    "v_rcp_f32 v137, v137\n"
    "v_mfma_f32_16x16x32_bf16 v[76:79], a[4:7], v[100:103], v[76:79]\n"
    "v_rcp_f32 v138, v138\n"
    "v_mfma_f32_16x16x32_bf16 v[80:83], a[8:11], v[100:103], v[80:83]\n"
    "v_rcp_f32 v139, v139\n"
    "v_mfma_f32_16x16x32_bf16 v[84:87], a[12:15], v[100:103], v[84:87]\n"
    "ds_read_b128 v[100:103], v4 offset:52224\n"
    "v_fma_f32 v68, -v68, v136, v68\n"
    "v_fma_f32 v69, -v69, v137, v69\n"
    "s_waitcnt lgkmcnt(2)\n"
    "v_mfma_f32_16x16x32_bf16 v[76:79], a[16:19], v[104:107], v[76:79]\n"
    "v_fma_f32 v70, -v70, v138, v70\n"
    "v_fma_f32 v71, -v71, v139, v71\n"
    "v_mfma_f32_16x16x32_bf16 v[80:83], a[20:23], v[104:107], v[80:83]\n"
    "v_cvt_pk_bf16_f32 v124, v68, v69\n"
    "v_cvt_pk_bf16_f32 v125, v70, v71\n"
    "v_mfma_f32_16x16x32_bf16 v[84:87], a[24:27], v[104:107], v[84:87]\n"
    "v_mul_f32 v72, v72, v144\n"
    "v_mul_f32 v73, v73, v144\n"
    "v_mul_f32 v74, v74, v144\n"
    "v_mfma_f32_16x16x32_bf16 v[76:79], a[28:31], v[108:111], v[76:79]\n"
    "v_mul_f32 v75, v75, v144\n"
    "v_exp_f32 v132, v72\n"
    "v_mfma_f32_16x16x32_bf16 v[80:83], a[32:35], v[108:111], v[80:83]\n"
    "v_exp_f32 v133, v73\n"
    "v_mfma_f32_16x16x32_bf16 v[84:87], a[36:39], v[108:111], v[84:87]\n"
    "v_exp_f32 v134, v74\n"
    "v_mfma_f32_16x16x32_bf16 v[76:79], a[40:43], v[112:115], v[76:79]\n"
    "v_exp_f32 v135, v75\n"
    "v_mfma_f32_16x16x32_bf16 v[80:83], a[44:47], v[112:115], v[80:83]\n"
    "v_fma_f32 v136, v132, 0.5, 1.0\n"
    "v_fma_f32 v137, v133, 0.5, 1.0\n"
    "v_mfma_f32_16x16x32_bf16 v[84:87], a[48:51], v[112:115], v[84:87]\n"
    "v_fma_f32 v138, v134, 0.5, 1.0\n"
    "v_fma_f32 v139, v135, 0.5, 1.0\n"
    "v_mfma_f32_16x16x32_bf16 v[76:79], a[52:55], v[116:119], v[76:79]\n"
    "s_mov_b64 exec, 0xff\n"
    "s_waitcnt vmcnt(4)\n"
    "v_cmp_ne_u32 vcc, s98, v60\n"
    "s_mov_b64 exec, -1\n"
    "s_cbranch_vccz .Lpd5\n"
    "s_mov_b64 exec, 255\n"
    ".Lps5:\n"
    "s_sleep 1\n"
    "buffer_load_dword v60, v11, s[16:19], s82 offen sc1\n"
    "s_waitcnt vmcnt(0)\n"
    "v_cmp_ne_u32 vcc, s98, v60\n"
    "s_nop 1\n"
    "s_cbranch_vccz .Lpe5\n"
    "s_sub_u32 s67, s67, 1\n"
    "s_cbranch_scc0 .Lps5\n"
    "s_mov_b32 s67, 0\n"
    ".Lpe5:\n"
    "s_mov_b64 exec, -1\n"
    ".Lpd5:\n"
    "s_add_u32 m0, s53, 0\n"
    "s_add_u32 s87, s37, 0xc00000\n"
    "v_add_u32 v62, s87, v1\n"
    "global_load_lds_dwordx4 v62, s[16:17] sc1\n"
    "global_load_lds_dwordx4 v62, s[16:17] offset:1024 sc1\n"
    "global_load_lds_dwordx4 v62, s[16:17] offset:2048 sc1\n"
    "global_load_lds_dwordx4 v62, s[16:17] offset:3072 sc1\n"
    "v_fma_f32 v136, v132, v136, 1.0\n"
    "v_mfma_f32_16x16x32_bf16 v[80:83], a[56:59], v[116:119], v[80:83]\n"
    "v_fma_f32 v137, v133, v137, 1.0\n"
    "v_fma_f32 v138, v134, v138, 1.0\n"
    "v_fma_f32 v139, v135, v139, 1.0\n"
    "v_mfma_f32_16x16x32_bf16 v[84:87], a[60:63], v[116:119], v[84:87]\n"
    "v_rcp_f32 v136, v136\n"
    "s_waitcnt lgkmcnt(0)\n"
    "v_mfma_f32_16x16x32_bf16 v[76:79], a[64:67], v[96:99], v[76:79]\n"
    "v_rcp_f32 v137, v137\n"
    "v_mfma_f32_16x16x32_bf16 v[80:83], a[68:71], v[96:99], v[80:83]\n"
    "v_rcp_f32 v138, v138\n"
    "v_mfma_f32_16x16x32_bf16 v[84:87], a[72:75], v[96:99], v[84:87]\n"
    "v_rcp_f32 v139, v139\n"
    "v_mfma_f32_16x16x32_bf16 v[76:79], a[76:79], v[100:103], v[76:79]\n"
    "v_fma_f32 v72, -v72, v136, v72\n"
    "v_fma_f32 v73, -v73, v137, v73\n"
    "s_add_u32 m0, s53, 0x1000\n"
    "s_add_u32 s87, s37, 0xc01000\n"
    "v_add_u32 v62, s87, v1\n"
    "global_load_lds_dwordx4 v62, s[16:17] sc1\n"
    "global_load_lds_dwordx4 v62, s[16:17] offset:1024 sc1\n"
    "global_load_lds_dwordx4 v62, s[16:17] offset:2048 sc1\n"
    "global_load_lds_dwordx4 v62, s[16:17] offset:3072 sc1\n"
    "v_mfma_f32_16x16x32_bf16 v[80:83], a[80:83], v[100:103], v[80:83]\n"
    "v_fma_f32 v74, -v74, v138, v74\n"
    "v_fma_f32 v75, -v75, v139, v75\n"
    "v_mfma_f32_16x16x32_bf16 v[84:87], a[84:87], v[100:103], v[84:87]\n"
    "v_cvt_pk_bf16_f32 v128, v72, v73\n"
    "v_cvt_pk_bf16_f32 v129, v74, v75\n"
    "v_mul_f32 v76, v76, v145\n"
    "v_mul_f32 v77, v77, v145\n"
    "v_mul_f32 v78, v78, v145\n"
    "v_mul_f32 v79, v79, v145\n"
    "v_exp_f32 v132, v76\n"
    "v_exp_f32 v133, v77\n"
    "v_exp_f32 v134, v78\n"
    "v_exp_f32 v135, v79\n"
    "v_fma_f32 v136, v132, 0.5, 1.0\n"
    "v_fma_f32 v137, v133, 0.5, 1.0\n"
    "v_fma_f32 v138, v134, 0.5, 1.0\n"
    "v_fma_f32 v139, v135, 0.5, 1.0\n"
    "v_fma_f32 v136, v132, v136, 1.0\n"
    "v_fma_f32 v137, v133, v137, 1.0\n"
    "v_fma_f32 v138, v134, v138, 1.0\n"
    "v_fma_f32 v139, v135, v139, 1.0\n"
    "v_rcp_f32 v136, v136\n"
    "v_rcp_f32 v137, v137\n"
    "v_rcp_f32 v138, v138\n"
    "v_rcp_f32 v139, v139\n"
    "v_fma_f32 v76, -v76, v136, v76\n"
    "v_fma_f32 v77, -v77, v137, v77\n"
    "v_fma_f32 v78, -v78, v138, v78\n"
    "v_fma_f32 v79, -v79, v139, v79\n"
    "v_cvt_pk_bf16_f32 v122, v76, v77\n"
    "v_cvt_pk_bf16_f32 v123, v78, v79\n"
    "s_nop 0\n"
    "v_permlane16_swap_b32 v120, v122\n"
    "v_permlane16_swap_b32 v121, v123\n"
    "s_add_u32 s84, s45, 0xc16000\n"
    "buffer_store_dwordx4 v[120:123], v1, s[16:19], s84 offen sc1\n"
    "v_mul_f32 v80, v80, v145\n"
    "v_mul_f32 v81, v81, v145\n"
    "v_mul_f32 v82, v82, v145\n"
    "v_mul_f32 v83, v83, v145\n"
    "v_exp_f32 v132, v80\n"
    "v_exp_f32 v133, v81\n"
    "v_exp_f32 v134, v82\n"
    "v_exp_f32 v135, v83\n"
    "v_fma_f32 v136, v132, 0.5, 1.0\n"
    "v_fma_f32 v137, v133, 0.5, 1.0\n"
    "v_fma_f32 v138, v134, 0.5, 1.0\n"
    "v_fma_f32 v139, v135, 0.5, 1.0\n"
    "v_fma_f32 v136, v132, v136, 1.0\n"
    "v_fma_f32 v137, v133, v137, 1.0\n"
    "v_fma_f32 v138, v134, v138, 1.0\n"
    "v_fma_f32 v139, v135, v139, 1.0\n"
    "v_rcp_f32 v136, v136\n"
    "v_rcp_f32 v137, v137\n"
    "v_rcp_f32 v138, v138\n"
    "v_rcp_f32 v139, v139\n"
    "s_add_u32 m0, s53, 0x2000\n"
    "s_add_u32 s87, s37, 0xc02000\n"
    "v_add_u32 v62, s87, v1\n"
    "global_load_lds_dwordx4 v62, s[16:17] sc1\n"
    "global_load_lds_dwordx4 v62, s[16:17] offset:1024 sc1\n"
    "global_load_lds_dwordx4 v62, s[16:17] offset:2048 sc1\n"
    "global_load_lds_dwordx4 v62, s[16:17] offset:3072 sc1\n"
    "v_fma_f32 v80, -v80, v136, v80\n"
    "v_fma_f32 v81, -v81, v137, v81\n"
    "v_fma_f32 v82, -v82, v138, v82\n"
    "v_fma_f32 v83, -v83, v139, v83\n"
    "v_cvt_pk_bf16_f32 v126, v80, v81\n"
    "v_cvt_pk_bf16_f32 v127, v82, v83\n"
    "s_nop 0\n"
    "v_permlane16_swap_b32 v124, v126\n"
    "v_permlane16_swap_b32 v125, v127\n"
    "buffer_store_dwordx4 v[124:127], v1, s[16:19], s84 offen offset:1024 sc1\n"
    "v_mul_f32 v84, v84, v145\n"
    "v_mul_f32 v85, v85, v145\n"
    "v_mul_f32 v86, v86, v145\n"
    "v_mul_f32 v87, v87, v145\n"
    "v_exp_f32 v132, v84\n"
    "v_exp_f32 v133, v85\n"
    "v_exp_f32 v134, v86\n"
    "v_exp_f32 v135, v87\n"
    "v_fma_f32 v136, v132, 0.5, 1.0\n"
    "v_fma_f32 v137, v133, 0.5, 1.0\n"
    "v_fma_f32 v138, v134, 0.5, 1.0\n"
    "v_fma_f32 v139, v135, 0.5, 1.0\n"
    "v_fma_f32 v136, v132, v136, 1.0\n"
    "v_fma_f32 v137, v133, v137, 1.0\n"
    "v_fma_f32 v138, v134, v138, 1.0\n"
    "v_fma_f32 v139, v135, v139, 1.0\n"
    "v_rcp_f32 v136, v136\n"
    "v_rcp_f32 v137, v137\n"
    "v_rcp_f32 v138, v138\n"
    "v_rcp_f32 v139, v139\n"
    "v_fma_f32 v84, -v84, v136, v84\n"
    "v_fma_f32 v85, -v85, v137, v85\n"
    "v_add_u32 v61, 0x2000, v61\n"
    "global_load_dwordx4 a[240:243], v61, s[16:17] offset:-4096\n"
    "global_load_dwordx4 a[248:251], v61, s[16:17] offset:-3072\n"
    "v_fma_f32 v86, -v86, v138, v86\n"
    "global_load_dwordx4 v[152:155], v61, s[16:17] offset:-2048\n"
    "global_load_dwordx4 v[156:159], v61, s[16:17] offset:-1024\n"
    "v_fma_f32 v87, -v87, v139, v87\n"
    "v_cvt_pk_bf16_f32 v130, v84, v85\n"
    "v_cvt_pk_bf16_f32 v131, v86, v87\n"
    "s_nop 0\n"
    "v_permlane16_swap_b32 v128, v130\n"
    "v_permlane16_swap_b32 v129, v131\n"
    "buffer_store_dwordx4 v[128:131], v151, s[16:19], s84 offen offset:2048 sc1\n"
    "s_waitcnt vmcnt(15)\n"
    "ds_read_b128 v[96:99], v6 offset:0\n"
    "ds_read_b128 v[100:103], v6 offset:1024\n"
    "ds_read_b128 v[104:107], v6 offset:2048\n"
    "ds_read_b128 v[108:111], v6 offset:3072\n"
    "s_add_u32 s64, s57, 0x2400\n"
    "v_add_u32 v120, s64, v1\n"
    "s_add_u32 s64, s58, 0x1400\n"
    "v_add_u32 v121, s64, v1\n"
    "s_add_u32 s64, s59, 0x1c00\n"
    "v_add_u32 v122, s64, v1\n"
    "s_add_u32 m0, s53, 0x3000\n"
    "s_add_u32 s87, s37, 0xc03000\n"
    "v_add_u32 v62, s87, v1\n"
    "global_load_lds_dwordx4 v62, s[16:17] sc1\n"
    "global_load_lds_dwordx4 v62, s[16:17] offset:1024 sc1\n"
    "global_load_lds_dwordx4 v62, s[16:17] offset:2048 sc1\n"
    "global_load_lds_dwordx4 v62, s[16:17] offset:3072 sc1\n"
    "s_waitcnt lgkmcnt(0)\n"
    "v_mfma_f32_32x32x16_bf16 v[64:79], a[88:91], v[96:99], 0\n"
    "v_mfma_f32_32x32x16_bf16 v[80:95], a[92:95], v[96:99], 0\n"
    "v_mfma_f32_32x32x16_bf16 v[64:79], a[96:99], v[100:103], v[64:79]\n"
    "v_mfma_f32_32x32x16_bf16 v[80:95], a[100:103], v[100:103], v[80:95]\n"
    "v_mfma_f32_32x32x16_bf16 v[64:79], a[104:107], v[104:107], v[64:79]\n"
    "s_waitcnt vmcnt(14)\n"
    "ds_read_b128 v[112:115], v6 offset:4096\n"
    "ds_read_b128 v[116:119], v6 offset:5120\n"
    "ds_read_b128 v[96:99], v6 offset:6144\n"
    "v_mfma_f32_32x32x16_bf16 v[80:95], a[108:111], v[104:107], v[80:95]\n"
    "ds_read_b128 v[100:103], v6 offset:7168\n"
    "v_mfma_f32_32x32x16_bf16 v[64:79], a[112:115], v[108:111], v[64:79]\n"
    "v_mfma_f32_32x32x16_bf16 v[80:95], a[116:119], v[108:111], v[80:95]\n"
    "s_add_u32 m0, s53, 0x4000\n"
    "s_add_u32 s87, s37, 0xc04000\n"
    "v_add_u32 v62, s87, v1\n"
    "global_load_lds_dwordx4 v62, s[16:17] sc1\n"
    "global_load_lds_dwordx4 v62, s[16:17] offset:1024 sc1\n"
    "global_load_lds_dwordx4 v62, s[16:17] offset:2048 sc1\n"
    "global_load_lds_dwordx4 v62, s[16:17] offset:3072 sc1\n"
    "s_waitcnt lgkmcnt(0)\n"
    "v_mfma_f32_32x32x16_bf16 v[64:79], a[120:123], v[112:115], v[64:79]\n"
    "v_mfma_f32_32x32x16_bf16 v[80:95], a[124:127], v[112:115], v[80:95]\n"
    "v_mfma_f32_32x32x16_bf16 v[64:79], a[128:131], v[116:119], v[64:79]\n"
    "v_mfma_f32_32x32x16_bf16 v[80:95], a[132:135], v[116:119], v[80:95]\n"
    "v_mfma_f32_32x32x16_bf16 v[64:79], a[136:139], v[96:99], v[64:79]\n"
    "s_waitcnt vmcnt(13)\n"
    "ds_read_b128 v[104:107], v6 offset:8192\n"
    "v_mfma_f32_32x32x16_bf16 v[80:95], a[140:143], v[96:99], v[80:95]\n"
    "ds_read_b128 v[108:111], v6 offset:9216\n"
    "ds_read_b128 v[112:115], v6 offset:10240\n"
    "ds_read_b128 v[116:119], v6 offset:11264\n"
    "s_add_u32 m0, s53, 0x5000\n"
    "s_add_u32 s87, s37, 0xc05000\n"
    "v_add_u32 v62, s87, v1\n"
    "global_load_lds_dwordx4 v62, s[16:17] sc1\n"
    "global_load_lds_dwordx4 v62, s[16:17] offset:1024 sc1\n"
    "v_mfma_f32_32x32x16_bf16 v[64:79], a[144:147], v[100:103], v[64:79]\n"
    "v_mfma_f32_32x32x16_bf16 v[80:95], a[148:151], v[100:103], v[80:95]\n"
    "s_waitcnt lgkmcnt(3)\n"
    "v_mfma_f32_32x32x16_bf16 v[64:79], a[152:155], v[104:107], v[64:79]\n"
    "v_mfma_f32_32x32x16_bf16 v[80:95], a[156:159], v[104:107], v[80:95]\n"
    "v_add_u32 v61, s38, v1\n"
    "global_load_dwordx4 v[160:163], v61, s[16:17] offset:-4096\n"
    "s_waitcnt lgkmcnt(0)\n"
    "v_mfma_f32_32x32x16_bf16 v[64:79], a[160:163], v[108:111], v[64:79]\n"
    "v_mfma_f32_32x32x16_bf16 v[80:95], a[164:167], v[108:111], v[80:95]\n"
    "global_load_dwordx4 v[164:167], v61, s[16:17] offset:-3072\n"
    "v_mfma_f32_32x32x16_bf16 v[64:79], a[168:171], v[112:115], v[64:79]\n"
    "global_load_dwordx4 v[168:171], v61, s[16:17] offset:-2048\n"
    "s_waitcnt vmcnt(13)\n"
    "s_barrier\n"
    "v_mov_b32 v62, s98\n"
    "s_add_u32 s83, s47, 0x1f14080\n"
    "s_mov_b64 exec, s[68:69]\n"
    "buffer_store_dword v62, off, s[16:19], s83 sc1\n"
    "s_mov_b64 exec, -1\n"
    "global_load_dwordx4 v[172:175], v61, s[16:17] offset:-1024\n"
    "global_load_dwordx4 v[176:179], v61, s[16:17]\n"
    "global_load_dwordx4 v[180:183], v61, s[16:17] offset:1024\n"
    "global_load_dwordx4 v[184:187], v61, s[16:17] offset:2048\n"
    "s_waitcnt vmcnt(14)\n"
    "ds_read_b128 v[96:99], v6 offset:12288\n"
    "v_mfma_f32_32x32x16_bf16 v[80:95], a[172:175], v[112:115], v[80:95]\n"
    "ds_read_b128 v[100:103], v6 offset:13312\n"
    "ds_read_b128 v[104:107], v6 offset:14336\n"
    "ds_read_b128 v[108:111], v6 offset:15360\n"
    "global_load_dwordx4 v[188:191], v61, s[16:17] offset:3072\n"
    "v_mfma_f32_32x32x16_bf16 v[64:79], a[176:179], v[116:119], v[64:79]\n"
    "v_mfma_f32_32x32x16_bf16 v[80:95], a[180:183], v[116:119], v[80:95]\n"
    "v_add_u32 v61, 0x2000, v61\n"
    "global_load_dwordx4 v[192:195], v61, s[16:17] offset:-4096\n"
    "s_waitcnt lgkmcnt(3)\n"
    "v_mfma_f32_32x32x16_bf16 v[64:79], a[184:187], v[96:99], v[64:79]\n"
    "global_load_dwordx4 v[196:199], v61, s[16:17] offset:-3072\n"
    "v_mfma_f32_32x32x16_bf16 v[80:95], a[188:191], v[96:99], v[80:95]\n"
    "s_waitcnt lgkmcnt(0)\n"
    "v_mfma_f32_32x32x16_bf16 v[64:79], a[192:195], v[100:103], v[64:79]\n"
    "global_load_dwordx4 v[200:203], v61, s[16:17] offset:-2048\n"
    "v_mfma_f32_32x32x16_bf16 v[80:95], a[196:199], v[100:103], v[80:95]\n"
    "v_mfma_f32_32x32x16_bf16 v[64:79], a[200:203], v[104:107], v[64:79]\n"
    "global_load_dwordx4 v[204:207], v61, s[16:17] offset:-1024\n"
    "s_waitcnt vmcnt(15)\n"
    "ds_read_b128 v[112:115], v6 offset:16384\n"
    "v_mfma_f32_32x32x16_bf16 v[80:95], a[204:207], v[104:107], v[80:95]\n"
    "ds_read_b128 v[116:119], v6 offset:17408\n"
    "ds_read_b128 v[96:99], v6 offset:18432\n"
    "ds_read_b128 v[100:103], v6 offset:19456\n"
    "v_mfma_f32_32x32x16_bf16 v[64:79], a[208:211], v[108:111], v[64:79]\n"
    "global_load_dwordx4 v[208:211], v61, s[16:17]\n"
    "v_mfma_f32_32x32x16_bf16 v[80:95], a[212:215], v[108:111], v[80:95]\n"
    "s_mov_b32 s82, 0x1f14080\n"
    "s_mov_b64 exec, 0xff\n"
    "buffer_load_dword v60, v11, s[16:19], s82 offen sc1\n"
    "s_mov_b64 exec, -1\n"
    "s_waitcnt lgkmcnt(3)\n"
    "v_mfma_f32_32x32x16_bf16 v[80:95], a[220:223], v[112:115], v[80:95]\n"
    "s_waitcnt vmcnt(15)\n"
    "ds_read_b128 v[104:107], v6 offset:20480\n"
    "s_waitcnt lgkmcnt(1)\n"
    "v_mfma_f32_32x32x16_bf16 v[80:95], a[236:239], v[116:119], v[80:95]\n"
    "ds_read_b128 v[108:111], v6 offset:21504\n"
    "global_load_dwordx4 v[212:215], v61, s[16:17] offset:1024\n"
    "v_mfma_f32_32x32x16_bf16 v[80:95], a[252:255], v[96:99], v[80:95]\n"
    "v_mfma_f32_32x32x16_bf16 v[80:95], a[232:235], v[100:103], v[80:95]\n"
    "global_load_dwordx4 v[216:219], v61, s[16:17] offset:2048\n"
    "s_waitcnt lgkmcnt(1)\n"
    "v_mfma_f32_32x32x16_bf16 v[80:95], a[248:251], v[104:107], v[80:95]\n"
    "s_waitcnt lgkmcnt(0)\n"
    "v_mfma_f32_32x32x16_bf16 v[80:95], v[156:159], v[108:111], v[80:95]\n"
    "global_load_dwordx4 v[220:223], v61, s[16:17] offset:3072\n"
    "v_mfma_f32_32x32x16_bf16 v[64:79], a[216:219], v[112:115], v[64:79]\n"
    "s_nop 3\n"
    "ds_write_b128 v6, v[80:83] offset:5120\n"
    "v_mfma_f32_32x32x16_bf16 v[64:79], a[228:231], v[116:119], v[64:79]\n"
    "v_add_u32 v61, 0x2000, v61\n"
    "global_load_dwordx4 v[224:227], v61, s[16:17] offset:-4096\n"
    "v_mfma_f32_32x32x16_bf16 v[64:79], a[244:247], v[96:99], v[64:79]\n"
    "ds_write_b128 v6, v[84:87] offset:6144\n"
    "global_load_dwordx4 v[228:231], v61, s[16:17] offset:-3072\n"
    "v_mfma_f32_32x32x16_bf16 v[64:79], a[224:227], v[100:103], v[64:79]\n"
    "ds_write_b128 v6, v[88:91] offset:7168\n"
    "v_mfma_f32_32x32x16_bf16 v[64:79], a[240:243], v[104:107], v[64:79]\n"
    "v_mfma_f32_32x32x16_bf16 v[64:79], v[152:155], v[108:111], v[64:79]\n"
    "ds_write_b128 v6, v[92:95] offset:8192\n"
    "global_load_dwordx4 v[232:235], v61, s[16:17] offset:-2048\n"
    "s_nop 7\n"
    "s_nop 1\n"
    "ds_write_b128 v6, v[72:75] offset:9216\n"
    "ds_write_b128 v6, v[76:79] offset:10240\n"
    "v_fma_f32 v16, v64, s77, v16\n"
    "v_fma_f32 v17, v65, s77, v17\n"
    "v_fma_f32 v18, v66, s77, v18\n"
    "v_fma_f32 v19, v67, s77, v19\n"
    "v_fma_f32 v20, v68, s77, v20\n"
    "v_fma_f32 v21, v69, s77, v21\n"
    "v_fma_f32 v22, v70, s77, v22\n"
    "v_fma_f32 v23, v71, s77, v23\n"
    "s_waitcnt lgkmcnt(0)\n"
    "s_barrier\n"
    "ds_read_b128 v[104:107], v121 offset:0\n"
    "ds_read_b128 v[108:111], v121 offset:1024\n"
    "ds_read_b128 v[112:115], v122 offset:0\n"
    "ds_read_b128 v[116:119], v122 offset:1024\n"
    "ds_read_b128 v[96:99], v120 offset:0\n"
    "ds_read_b128 v[100:103], v120 offset:1024\n"
    "global_load_dwordx4 v[236:239], v61, s[16:17] offset:-1024\n"
    "global_load_dwordx4 v[240:243], v61, s[16:17]\n"
    "global_load_dwordx4 v[244:247], v61, s[16:17] offset:1024\n"
    "global_load_dwordx4 v[248:251], v61, s[16:17] offset:2048\n"
    "s_waitcnt lgkmcnt(4)\n"
    "v_fma_f32 v16, v104, s77, v16\n"
    "v_fma_f32 v17, v105, s77, v17\n"
    "v_fma_f32 v18, v106, s77, v18\n"
    "v_fma_f32 v19, v107, s77, v19\n"
    "v_fma_f32 v20, v108, s77, v20\n"
    "v_fma_f32 v21, v109, s77, v21\n"
    "v_fma_f32 v22, v110, s77, v22\n"
    "v_fma_f32 v23, v111, s77, v23\n"
    "s_waitcnt lgkmcnt(0)\n"
    "v_fma_f32 v16, v112, s77, v16\n"
    "v_fma_f32 v17, v113, s77, v17\n"
    "v_fma_f32 v18, v114, s77, v18\n"
    "v_fma_f32 v19, v115, s77, v19\n"
    "v_fma_f32 v20, v116, s77, v20\n"
    "v_fma_f32 v21, v117, s77, v21\n"
    "v_fma_f32 v22, v118, s77, v22\n"
    "v_fma_f32 v23, v119, s77, v23\n"
    "v_fma_f32 v16, v96, s77, v16\n"
    "v_fma_f32 v17, v97, s77, v17\n"
    "v_fma_f32 v18, v98, s77, v18\n"
    "v_fma_f32 v19, v99, s77, v19\n"
    "v_fma_f32 v20, v100, s77, v20\n"
    "v_fma_f32 v21, v101, s77, v21\n"
    "v_fma_f32 v22, v102, s77, v22\n"
    "v_fma_f32 v23, v103, s77, v23\n"
    "v_cvt_pk_bf16_f32 v124, v16, v17\n"
    "v_cvt_pk_bf16_f32 v125, v18, v19\n"
    "v_cvt_pk_bf16_f32 v126, v20, v21\n"
    "v_cvt_pk_bf16_f32 v127, v22, v23\n"
    "s_add_u32 s84, s42, 0x1180000\n"
    "buffer_store_dwordx4 v[124:127], v8, s[16:19], s84 offen sc1\n"
    "v_add_f32 v128, v16, v17\n"
    "v_mul_f32 v129, v16, v16\n"
    "v_add_f32 v130, v18, v19\n"
    "v_mul_f32 v131, v17, v17\n"
    "v_fma_f32 v129, v18, v18, v129\n"
    "v_fma_f32 v131, v19, v19, v131\n"
    "v_add_f32 v128, v128, v20\n"
    "v_add_f32 v130, v130, v21\n"
    "v_fma_f32 v129, v20, v20, v129\n"
    "v_fma_f32 v131, v21, v21, v131\n"
    "v_add_f32 v128, v128, v22\n"
    "v_add_f32 v130, v130, v23\n"
    "v_fma_f32 v129, v22, v22, v129\n"
    "v_fma_f32 v131, v23, v23, v131\n"
    "v_add_f32 v128, v128, v130\n"
    "v_add_f32 v129, v129, v131\n"
    "s_add_u32 s85, s43, 0x1380000\n"
    "global_load_dwordx4 v[252:255], v61, s[16:17] offset:3072\n"
    "v_permlane32_swap_b32 v128, v129\n"
    "v_add_f32 v128, v128, v129\n"
    "buffer_store_dword v128, v9, s[16:19], s85 offen sc1\n"
    "v_add_u32 v61, 0x2000, v61\n"
    "global_load_dwordx4 a[0:3], v61, s[16:17] offset:-4096\n"
    "s_mov_b64 exec, 0xff\n"
    "s_waitcnt vmcnt(14)\n"
    "v_cmp_ne_u32 vcc, s98, v60\n"
    "s_mov_b64 exec, -1\n"
    "s_cbranch_vccz .Lpd6\n"
    "s_mov_b64 exec, 255\n"
    ".Lps6:\n"
    "s_sleep 1\n"
    "buffer_load_dword v60, v11, s[16:19], s82 offen sc1\n"
    "s_waitcnt vmcnt(0)\n"
    "v_cmp_ne_u32 vcc, s98, v60\n"
    "s_nop 1\n"
    "s_cbranch_vccz .Lpe6\n"
    "s_sub_u32 s67, s67, 1\n"
    "s_cbranch_scc0 .Lps6\n"
    "s_mov_b32 s67, 0\n"
    ".Lpe6:\n"
    "s_mov_b64 exec, -1\n"
    ".Lpd6:\n"
    "s_add_u32 m0, s52, 0x16000\n"
    "s_add_u32 s87, s37, 0xc16000\n"
    "v_add_u32 v62, s87, v1\n"
    "global_load_lds_dwordx4 v62, s[16:17] sc1\n"
    "global_load_lds_dwordx4 v62, s[16:17] offset:1024 sc1\n"
    "global_load_lds_dwordx4 v62, s[16:17] offset:2048 sc1\n"
    "global_load_lds_dwordx4 v62, s[16:17] offset:3072 sc1\n"
    "s_add_u32 m0, s52, 0x17000\n"
    "s_add_u32 s87, s37, 0xc17000\n"
    "v_add_u32 v62, s87, v1\n"
    "global_load_lds_dwordx4 v62, s[16:17] sc1\n"
    "global_load_lds_dwordx4 v62, s[16:17] offset:1024 sc1\n"
    "global_load_lds_dwordx4 v62, s[16:17] offset:2048 sc1\n"
    "global_load_lds_dwordx4 v62, s[16:17] offset:3072 sc1\n"
    "s_add_u32 m0, s52, 0x18000\n"
    "s_add_u32 s87, s37, 0xc18000\n"
    "v_add_u32 v62, s87, v1\n"
    "global_load_lds_dwordx4 v62, s[16:17] sc1\n"
    "global_load_lds_dwordx4 v62, s[16:17] offset:1024 sc1\n"
    "global_load_lds_dwordx4 v62, s[16:17] offset:2048 sc1\n"
    "global_load_lds_dwordx4 v62, s[16:17] offset:3072 sc1\n"
    "s_waitcnt vmcnt(13)\n"
    "s_barrier\n"
    "v_mov_b32 v62, s98\n"
    "s_add_u32 s83, s47, 0x1f16000\n"
    "s_mov_b64 exec, s[68:69]\n"
    "buffer_store_dword v62, off, s[16:19], s83 sc1\n"
    "s_mov_b64 exec, -1\n"
    "s_add_u32 m0, s52, 0x19000\n"
    "s_add_u32 s87, s37, 0xc19000\n"
    "v_add_u32 v62, s87, v1\n"
    "global_load_lds_dwordx4 v62, s[16:17] sc1\n"
    "global_load_lds_dwordx4 v62, s[16:17] offset:1024 sc1\n"
    "global_load_lds_dwordx4 v62, s[16:17] offset:2048 sc1\n"
    "global_load_lds_dwordx4 v62, s[16:17] offset:3072 sc1\n"
    "global_load_dwordx4 a[4:7], v61, s[16:17] offset:-3072\n"
    "s_waitcnt vmcnt(14)\n"
    "ds_read_b128 v[96:99], v7 offset:0\n"
    "ds_read_b128 v[100:103], v7 offset:1024\n"
    "ds_read_b128 v[104:107], v7 offset:2048\n"
    "ds_read_b128 v[108:111], v7 offset:3072\n"
    "s_add_u32 s64, s60, 0x17000\n"
    "v_add_u32 v120, s64, v1\n"
    "s_add_u32 s64, s61, 0x16000\n"
    "v_add_u32 v121, s64, v1\n"
    "s_add_u32 s64, s62, 0x16800\n"
    "v_add_u32 v122, s64, v1\n"
    "s_add_u32 m0, s52, 0x1a000\n"
    "s_add_u32 s87, s37, 0xc1a000\n"
    "v_add_u32 v62, s87, v1\n"
    "global_load_lds_dwordx4 v62, s[16:17] sc1\n"
    "s_waitcnt lgkmcnt(2)\n"
    "v_mfma_f32_32x32x16_bf16 v[64:79], a[88:91], v[96:99], 0\n"
    "s_add_u32 m0, s53, 0\n"
    "s_add_u32 s87, s37, 0xc1a400\n"
    "v_add_u32 v62, s87, v1\n"
    "global_load_lds_dwordx4 v62, s[16:17] sc1\n"
    "global_load_lds_dwordx4 v62, s[16:17] offset:1024 sc1\n"
    "global_load_lds_dwordx4 v62, s[16:17] offset:2048 sc1\n"
    "global_load_lds_dwordx4 v62, s[16:17] offset:3072 sc1\n"
    "v_mfma_f32_32x32x16_bf16 v[80:95], a[92:95], v[96:99], 0\n"
    "v_mfma_f32_32x32x16_bf16 v[64:79], a[96:99], v[100:103], v[64:79]\n"
    "v_mfma_f32_32x32x16_bf16 v[80:95], a[100:103], v[100:103], v[80:95]\n"
    "s_waitcnt lgkmcnt(0)\n"
    "v_mfma_f32_32x32x16_bf16 v[64:79], a[104:107], v[104:107], v[64:79]\n"
    "s_waitcnt vmcnt(15)\n"
    "ds_read_b128 v[112:115], v7 offset:4096\n"
    "ds_read_b128 v[116:119], v7 offset:5120\n"
    "ds_read_b128 v[96:99], v7 offset:6144\n"
    "ds_read_b128 v[100:103], v7 offset:7168\n"
    "s_mov_b32 s82, 0x1f16000\n"
    "s_mov_b64 exec, 0xffff\n"
    "buffer_load_dword v60, v11, s[16:19], s82 offen sc1\n"
    "s_mov_b64 exec, -1\n"
    "v_mfma_f32_32x32x16_bf16 v[80:95], a[108:111], v[104:107], v[80:95]\n"
    "v_mfma_f32_32x32x16_bf16 v[64:79], a[112:115], v[108:111], v[64:79]\n"
    "v_mfma_f32_32x32x16_bf16 v[80:95], a[116:119], v[108:111], v[80:95]\n"
    "s_waitcnt lgkmcnt(2)\n"
    "v_mfma_f32_32x32x16_bf16 v[64:79], a[120:123], v[112:115], v[64:79]\n"
    "v_mfma_f32_32x32x16_bf16 v[80:95], a[124:127], v[112:115], v[80:95]\n"
    "v_mfma_f32_32x32x16_bf16 v[64:79], a[128:131], v[116:119], v[64:79]\n"
    "s_add_u32 m0, s53, 0x1000\n"
    "s_add_u32 s87, s37, 0xc1b400\n"
    "v_add_u32 v62, s87, v1\n"
    "global_load_lds_dwordx4 v62, s[16:17] sc1\n"
    "v_mfma_f32_32x32x16_bf16 v[80:95], a[132:135], v[116:119], v[80:95]\n"
    "s_waitcnt lgkmcnt(0)\n"
    "v_mfma_f32_32x32x16_bf16 v[64:79], a[136:139], v[96:99], v[64:79]\n"
    "global_load_dwordx4 a[8:11], v61, s[16:17] offset:-2048\n"
    "s_waitcnt vmcnt(13)\n"
    "ds_read_b128 v[104:107], v7 offset:8192\n"
    "v_mfma_f32_32x32x16_bf16 v[80:95], a[140:143], v[96:99], v[80:95]\n"
    "ds_read_b128 v[108:111], v7 offset:9216\n"
    "ds_read_b128 v[112:115], v7 offset:10240\n"
    "ds_read_b128 v[116:119], v7 offset:11264\n"
    "v_mfma_f32_32x32x16_bf16 v[64:79], a[144:147], v[100:103], v[64:79]\n"
    "global_load_dwordx4 a[12:15], v61, s[16:17] offset:-1024\n"
    "v_mfma_f32_32x32x16_bf16 v[80:95], a[148:151], v[100:103], v[80:95]\n"
    "s_waitcnt lgkmcnt(3)\n"
    "v_mfma_f32_32x32x16_bf16 v[64:79], a[152:155], v[104:107], v[64:79]\n"
    "global_load_dwordx4 a[16:19], v61, s[16:17]\n"
    "v_mfma_f32_32x32x16_bf16 v[80:95], a[156:159], v[104:107], v[80:95]\n"
    "s_waitcnt lgkmcnt(0)\n"
    "v_mfma_f32_32x32x16_bf16 v[64:79], a[160:163], v[108:111], v[64:79]\n"
    "global_load_dwordx4 a[20:23], v61, s[16:17] offset:1024\n"
    "v_mfma_f32_32x32x16_bf16 v[80:95], a[164:167], v[108:111], v[80:95]\n"
    "v_mfma_f32_32x32x16_bf16 v[64:79], a[168:171], v[112:115], v[64:79]\n"
    "global_load_dwordx4 a[24:27], v61, s[16:17] offset:2048\n"
    "global_load_dwordx4 a[28:31], v61, s[16:17] offset:3072\n"
    "s_waitcnt vmcnt(14)\n"
    "ds_read_b128 v[96:99], v7 offset:12288\n"
    "v_mfma_f32_32x32x16_bf16 v[80:95], a[172:175], v[112:115], v[80:95]\n"
    "ds_read_b128 v[100:103], v7 offset:13312\n"
    "ds_read_b128 v[104:107], v7 offset:14336\n"
    "ds_read_b128 v[108:111], v7 offset:15360\n"
    "v_add_u32 v61, 0x2000, v61\n"
    "global_load_dwordx4 a[32:35], v61, s[16:17] offset:-4096\n"
    "v_mfma_f32_32x32x16_bf16 v[64:79], a[176:179], v[116:119], v[64:79]\n"
    "v_mfma_f32_32x32x16_bf16 v[80:95], a[180:183], v[116:119], v[80:95]\n"
    "global_load_dwordx4 a[36:39], v61, s[16:17] offset:-3072\n"
    "s_waitcnt lgkmcnt(3)\n"
    "v_mfma_f32_32x32x16_bf16 v[64:79], a[184:187], v[96:99], v[64:79]\n"
    "global_load_dwordx4 a[40:43], v61, s[16:17] offset:-2048\n"
    "v_mfma_f32_32x32x16_bf16 v[80:95], a[188:191], v[96:99], v[80:95]\n"
    "s_waitcnt lgkmcnt(0)\n"
    "v_mfma_f32_32x32x16_bf16 v[64:79], a[192:195], v[100:103], v[64:79]\n"
    "global_load_dwordx4 a[44:47], v61, s[16:17] offset:-1024\n"
    "v_mfma_f32_32x32x16_bf16 v[80:95], a[196:199], v[100:103], v[80:95]\n"
    "v_mfma_f32_32x32x16_bf16 v[64:79], a[200:203], v[104:107], v[64:79]\n"
    "global_load_dwordx4 a[48:51], v61, s[16:17]\n"
    "global_load_dwordx4 a[52:55], v61, s[16:17] offset:1024\n"
    "s_waitcnt vmcnt(15)\n"
    "ds_read_b128 v[112:115], v7 offset:16384\n"
    "v_mfma_f32_32x32x16_bf16 v[80:95], a[204:207], v[104:107], v[80:95]\n"
    "ds_read_b128 v[116:119], v6 offset:0\n"
    "ds_read_b128 v[96:99], v6 offset:1024\n"
    "ds_read_b128 v[100:103], v6 offset:2048\n"
    "v_mfma_f32_32x32x16_bf16 v[64:79], a[208:211], v[108:111], v[64:79]\n"
    "global_load_dwordx4 a[56:59], v61, s[16:17] offset:2048\n"
    "v_mfma_f32_32x32x16_bf16 v[80:95], a[212:215], v[108:111], v[80:95]\n"
    "global_load_dwordx4 a[60:63], v61, s[16:17] offset:3072\n"
    "s_waitcnt lgkmcnt(3)\n"
    "v_mfma_f32_32x32x16_bf16 v[80:95], a[220:223], v[112:115], v[80:95]\n"
    "s_mov_b64 exec, 0xffff\n"
    "s_waitcnt vmcnt(15)\n"
    "v_cmp_ne_u32 vcc, s98, v60\n"
    "s_mov_b64 exec, -1\n"
    "s_cbranch_vccz .Lpd7\n"
    "s_mov_b64 exec, 65535\n"
    ".Lps7:\n"
    "s_sleep 1\n"
    "buffer_load_dword v60, v11, s[16:19], s82 offen sc1\n"
    "s_waitcnt vmcnt(0)\n"
    "v_cmp_ne_u32 vcc, s98, v60\n"
    "s_nop 1\n"
    "s_cbranch_vccz .Lpe7\n"
    "s_sub_u32 s67, s67, 1\n"
    "s_cbranch_scc0 .Lps7\n"
    "s_mov_b32 s67, 0\n"
    ".Lpe7:\n"
    "s_mov_b64 exec, -1\n"
    ".Lpd7:\n"
    "s_add_u32 m0, s48, 0x1400\n"
    "s_add_u32 s87, s95, 0x1180000\n"
    "v_add_u32 v62, s87, v148\n"
    "global_load_lds_dwordx4 v62, s[16:17] sc1\n"
    "s_waitcnt vmcnt(15)\n"
    "ds_read_b128 v[104:107], v6 offset:3072\n"
    "s_waitcnt lgkmcnt(1)\n"
    "v_mfma_f32_32x32x16_bf16 v[80:95], a[236:239], v[116:119], v[80:95]\n"
    "ds_read_b128 v[108:111], v6 offset:4096\n"
    "s_add_u32 m0, s49, 0x6c00\n"
    "s_add_u32 s87, s31, 0x1180000\n"
    "v_add_u32 v62, s87, v1\n"
    "global_load_lds_dwordx4 v62, s[16:17] sc1\n"
    "global_load_lds_dwordx4 v62, s[16:17] offset:1024 sc1\n"
    "v_mfma_f32_32x32x16_bf16 v[80:95], a[252:255], v[96:99], v[80:95]\n"
    "v_mfma_f32_32x32x16_bf16 v[80:95], a[232:235], v[100:103], v[80:95]\n"
    "s_waitcnt lgkmcnt(1)\n"
    "v_mfma_f32_32x32x16_bf16 v[80:95], a[248:251], v[104:107], v[80:95]\n"
    "s_waitcnt lgkmcnt(0)\n"
    "v_mfma_f32_32x32x16_bf16 v[80:95], v[156:159], v[108:111], v[80:95]\n"
    "s_add_u32 m0, s48, 0x2400\n"
    "s_add_u32 s87, s95, 0x1182000\n"
    "v_add_u32 v62, s87, v148\n"
    "global_load_lds_dwordx4 v62, s[16:17] sc1\n"
    "v_mfma_f32_32x32x16_bf16 v[64:79], a[216:219], v[112:115], v[64:79]\n"
    "s_nop 3\n"
    "ds_write_b128 v7, v[80:83] offset:0\n"
    "v_mfma_f32_32x32x16_bf16 v[64:79], a[228:231], v[116:119], v[64:79]\n"
    "s_add_u32 m0, s49, 0x8c00\n"
    "s_add_u32 s87, s31, 0x1182000\n"
    "v_add_u32 v62, s87, v1\n"
    "global_load_lds_dwordx4 v62, s[16:17] sc1\n"
    "global_load_lds_dwordx4 v62, s[16:17] offset:1024 sc1\n"
    "ds_write_b128 v7, v[84:87] offset:1024\n"
    "v_mfma_f32_32x32x16_bf16 v[64:79], a[244:247], v[96:99], v[64:79]\n"
    "v_mfma_f32_32x32x16_bf16 v[64:79], a[224:227], v[100:103], v[64:79]\n"
    "ds_write_b128 v7, v[88:91] offset:2048\n"
    "v_mfma_f32_32x32x16_bf16 v[64:79], a[240:243], v[104:107], v[64:79]\n"
    "ds_write_b128 v7, v[92:95] offset:3072\n"
    "v_mfma_f32_32x32x16_bf16 v[64:79], v[152:155], v[108:111], v[64:79]\n"
    "s_add_u32 m0, s48, 0x11c00\n"
    "s_add_u32 s87, s34, 0x1380000\n"
    "v_add_u32 v62, s87, v1\n"
    "global_load_lds_dwordx4 v62, s[16:17] sc1\n"
    "s_nop 7\n"
    "ds_write_b128 v7, v[72:75] offset:4096\n"
    "ds_write_b128 v7, v[76:79] offset:5120\n"
    "v_fma_f32 v24, v64, s77, v24\n"
    "s_add_u32 m0, s49, 0x12c00\n"
    "s_add_u32 s87, s35, 0x1380000\n"
    "v_add_u32 v62, s87, v1\n"
    "global_load_lds_dwordx4 v62, s[16:17] sc1\n"
    "global_load_lds_dwordx4 v62, s[16:17] offset:1024 sc1\n"
    "v_fma_f32 v25, v65, s77, v25\n"
    "v_fma_f32 v26, v66, s77, v26\n"
    "v_fma_f32 v27, v67, s77, v27\n"
    "v_fma_f32 v28, v68, s77, v28\n"
    "v_fma_f32 v29, v69, s77, v29\n"
    "v_fma_f32 v30, v70, s77, v30\n"
    "v_fma_f32 v31, v71, s77, v31\n"
    "s_waitcnt lgkmcnt(0)\n"
    "s_barrier\n"
    "ds_read_b128 v[104:107], v121 offset:0\n"
    "ds_read_b128 v[108:111], v121 offset:1024\n"
    "ds_read_b128 v[112:115], v122 offset:0\n"
    "ds_read_b128 v[116:119], v122 offset:1024\n"
    "ds_read_b128 v[96:99], v120 offset:0\n"
    "ds_read_b128 v[100:103], v120 offset:1024\n"
    "s_add_u32 m0, s50, 0xc400\n"
    "s_add_u32 s87, s96, 0x1180000\n"
    "v_add_u32 v62, s87, v1\n"
    "global_load_lds_dwordx4 v62, s[16:17] sc1\n"
    "global_load_lds_dwordx4 v62, s[16:17] offset:1024 sc1\n"
    "global_load_lds_dwordx4 v62, s[16:17] offset:2048 sc1\n"
    "global_load_lds_dwordx4 v62, s[16:17] offset:3072 sc1\n"
    "s_waitcnt lgkmcnt(4)\n"
    "v_fma_f32 v24, v104, s77, v24\n"
    "v_fma_f32 v25, v105, s77, v25\n"
    "v_fma_f32 v26, v106, s77, v26\n"
    "v_fma_f32 v27, v107, s77, v27\n"
    "v_fma_f32 v28, v108, s77, v28\n"
    "v_fma_f32 v29, v109, s77, v29\n"
    "v_fma_f32 v30, v110, s77, v30\n"
    "v_fma_f32 v31, v111, s77, v31\n"
    "s_waitcnt lgkmcnt(0)\n"
    "v_fma_f32 v24, v112, s77, v24\n"
    "v_fma_f32 v25, v113, s77, v25\n"
    "v_fma_f32 v26, v114, s77, v26\n"
    "v_fma_f32 v27, v115, s77, v27\n"
    "v_fma_f32 v28, v116, s77, v28\n"
    "v_fma_f32 v29, v117, s77, v29\n"
    "v_fma_f32 v30, v118, s77, v30\n"
    "v_fma_f32 v31, v119, s77, v31\n"
    "v_fma_f32 v24, v96, s77, v24\n"
    "v_fma_f32 v25, v97, s77, v25\n"
    "v_fma_f32 v26, v98, s77, v26\n"
    "v_fma_f32 v27, v99, s77, v27\n"
    "v_fma_f32 v28, v100, s77, v28\n"
    "v_fma_f32 v29, v101, s77, v29\n"
    "v_fma_f32 v30, v102, s77, v30\n"
    "v_fma_f32 v31, v103, s77, v31\n"
    "v_cvt_pk_bf16_f32 v124, v24, v25\n"
    "v_cvt_pk_bf16_f32 v125, v26, v27\n"
    "v_cvt_pk_bf16_f32 v126, v28, v29\n"
    "v_cvt_pk_bf16_f32 v127, v30, v31\n"
    "s_add_u32 s84, s42, 0x1188000\n"
    "buffer_store_dwordx4 v[124:127], v8, s[16:19], s84 offen sc1\n"
    "v_add_f32 v128, v24, v25\n"
    "v_mul_f32 v129, v24, v24\n"
    "v_add_f32 v130, v26, v27\n"
    "v_mul_f32 v131, v25, v25\n"
    "v_fma_f32 v129, v26, v26, v129\n"
    "v_fma_f32 v131, v27, v27, v131\n"
    "v_add_f32 v128, v128, v28\n"
    "v_add_f32 v130, v130, v29\n"
    "v_fma_f32 v129, v28, v28, v129\n"
    "v_fma_f32 v131, v29, v29, v131\n"
    "v_add_f32 v128, v128, v30\n"
    "v_add_f32 v130, v130, v31\n"
    "v_fma_f32 v129, v30, v30, v129\n"
    "v_fma_f32 v131, v31, v31, v131\n"
    "v_add_f32 v128, v128, v130\n"
    "v_add_f32 v129, v129, v131\n"
    "s_add_u32 s85, s43, 0x1382000\n"
    "s_nop 0\n"
    "v_permlane32_swap_b32 v128, v129\n"
    "v_add_f32 v128, v128, v129\n"
    "buffer_store_dword v128, v9, s[16:19], s85 offen sc1\n"
    "v_add_u32 v61, 0x2000, v61\n"
    "global_load_dwordx4 a[64:67], v61, s[16:17] offset:-4096\n"
    "global_load_dwordx4 a[68:71], v61, s[16:17] offset:-3072\n"
    "s_waitcnt vmcnt(14)\n"
    "s_barrier\n"
    "v_add_u32 v63, 0x1400, v149\n"
    "ds_read_b128 v[96:99], v63 offset:0\n"
    "ds_read_b128 v[100:103], v2 offset:27648\n"
    "ds_read_b128 v[104:107], v63 offset:512\n"
    "ds_read_b128 v[108:111], v2 offset:28672\n"
    "ds_read_b128 v[112:115], v63 offset:1024\n"
    "ds_read_b128 v[116:119], v2 offset:29696\n"
    "global_load_dwordx4 a[72:75], v61, s[16:17] offset:-2048\n"
    "s_waitcnt lgkmcnt(4)\n"
    "v_mfma_f32_16x16x32_bf16 v[64:67], v[164:167], v[96:99], 0\n"
    "global_load_dwordx4 a[76:79], v61, s[16:17] offset:-1024\n"
    "v_mfma_f32_16x16x32_bf16 v[72:75], v[160:163], v[100:103], 0\n"
    "v_mfma_f32_16x16x32_bf16 v[68:71], v[96:99], v[168:171], 0\n"
    "global_load_dwordx4 a[80:83], v61, s[16:17]\n"
    "v_mfma_f32_16x16x32_bf16 v[76:79], v[164:167], v[100:103], 0\n"
    "v_mfma_f32_16x16x32_bf16 v[80:83], v[100:103], v[168:171], 0\n"
    "ds_read_b128 v[96:99], v63 offset:1536\n"
    "s_waitcnt lgkmcnt(1)\n"
    "v_mfma_f32_16x16x32_bf16 v[64:67], v[176:179], v[104:107], v[64:67]\n"
    "ds_read_b128 v[100:103], v2 offset:30720\n"
    "v_mfma_f32_16x16x32_bf16 v[72:75], v[172:175], v[108:111], v[72:75]\n"
    "global_load_dwordx4 a[84:87], v61, s[16:17] offset:1024\n"
    "v_mfma_f32_16x16x32_bf16 v[68:71], v[104:107], v[180:183], v[68:71]\n"
    "v_mfma_f32_16x16x32_bf16 v[76:79], v[176:179], v[108:111], v[76:79]\n"
    "v_mfma_f32_16x16x32_bf16 v[80:83], v[108:111], v[180:183], v[80:83]\n"
    "ds_read_b128 v[104:107], v63 offset:2048\n"
    "ds_read_b128 v[108:111], v2 offset:31744\n"
    "v_mfma_f32_16x16x32_bf16 v[64:67], v[188:191], v[112:115], v[64:67]\n"
    "global_load_dwordx4 a[88:91], v61, s[16:17] offset:2048\n"
    "v_mfma_f32_16x16x32_bf16 v[72:75], v[184:187], v[116:119], v[72:75]\n"
    "v_mfma_f32_16x16x32_bf16 v[68:71], v[112:115], v[192:195], v[68:71]\n"
    "v_mfma_f32_16x16x32_bf16 v[76:79], v[188:191], v[116:119], v[76:79]\n"
    "v_mfma_f32_16x16x32_bf16 v[80:83], v[116:119], v[192:195], v[80:83]\n"
    "ds_read_b128 v[112:115], v63 offset:2560\n"
    "s_waitcnt lgkmcnt(3)\n"
    "v_mfma_f32_16x16x32_bf16 v[64:67], v[200:203], v[96:99], v[64:67]\n"
    "ds_read_b128 v[116:119], v2 offset:32768\n"
    "global_load_dwordx4 a[92:95], v61, s[16:17] offset:3072\n"
    "v_mfma_f32_16x16x32_bf16 v[72:75], v[196:199], v[100:103], v[72:75]\n"
    "v_mfma_f32_16x16x32_bf16 v[68:71], v[96:99], v[204:207], v[68:71]\n"
    "v_mfma_f32_16x16x32_bf16 v[76:79], v[200:203], v[100:103], v[76:79]\n"
    "v_add_u32 v61, s39, v1\n"
    "global_load_dwordx4 a[96:99], v61, s[16:17] offset:-4096\n"
    "v_mfma_f32_16x16x32_bf16 v[80:83], v[100:103], v[204:207], v[80:83]\n"
    "ds_read_b128 v[96:99], v63 offset:3072\n"
    "s_waitcnt lgkmcnt(3)\n"
    "v_mfma_f32_16x16x32_bf16 v[64:67], v[212:215], v[104:107], v[64:67]\n"
    "ds_read_b128 v[100:103], v2 offset:33792\n"
    "v_mfma_f32_16x16x32_bf16 v[72:75], v[208:211], v[108:111], v[72:75]\n"
    "global_load_dwordx4 a[100:103], v61, s[16:17] offset:-3072\n"
    "v_mfma_f32_16x16x32_bf16 v[68:71], v[104:107], v[216:219], v[68:71]\n"
    "v_mfma_f32_16x16x32_bf16 v[76:79], v[212:215], v[108:111], v[76:79]\n"
    "v_mfma_f32_16x16x32_bf16 v[80:83], v[108:111], v[216:219], v[80:83]\n"
    "ds_read_b128 v[104:107], v63 offset:3584\n"
    "s_waitcnt lgkmcnt(3)\n"
    "v_mfma_f32_16x16x32_bf16 v[64:67], v[224:227], v[112:115], v[64:67]\n"
    "ds_read_b128 v[108:111], v2 offset:34816\n"
    "global_load_dwordx4 a[104:107], v61, s[16:17] offset:-2048\n"
    "v_mfma_f32_16x16x32_bf16 v[72:75], v[220:223], v[116:119], v[72:75]\n"
    "v_mfma_f32_16x16x32_bf16 v[68:71], v[112:115], v[228:231], v[68:71]\n"
    "v_mfma_f32_16x16x32_bf16 v[76:79], v[224:227], v[116:119], v[76:79]\n"
    "v_mfma_f32_16x16x32_bf16 v[80:83], v[116:119], v[228:231], v[80:83]\n"
    "s_waitcnt vmcnt(15)\n"
    "s_barrier\n"
    "s_waitcnt lgkmcnt(2)\n"
    "v_mfma_f32_16x16x32_bf16 v[64:67], v[236:239], v[96:99], v[64:67]\n"
    "ds_read_b128 v[112:115], v63 offset:4096\n"
    "ds_read_b128 v[116:119], v2 offset:35840\n"
    "s_add_u32 s63, s70, 0x11c00\n"
    "v_add_u32 v147, s63, v12\n"
    "v_mfma_f32_16x16x32_bf16 v[72:75], v[232:235], v[100:103], v[72:75]\n"
    "ds_read_b64 v[128:129], v147 offset:0\n"
    "ds_read_b64 v[130:131], v147 offset:512\n"
    "v_mfma_f32_16x16x32_bf16 v[68:71], v[96:99], v[240:243], v[68:71]\n"
    "ds_read_b64 v[132:133], v147 offset:1024\n"
    "ds_read_b64 v[134:135], v147 offset:1536\n"
    "v_mfma_f32_16x16x32_bf16 v[76:79], v[236:239], v[100:103], v[76:79]\n"
    "ds_read_b64 v[136:137], v147 offset:2048\n"
    "ds_read_b64 v[138:139], v147 offset:2560\n"
    "v_mfma_f32_16x16x32_bf16 v[80:83], v[100:103], v[240:243], v[80:83]\n"
    "ds_read_b128 v[96:99], v63 offset:4608\n"
    "s_waitcnt lgkmcnt(9)\n"
    "v_mfma_f32_16x16x32_bf16 v[64:67], v[248:251], v[104:107], v[64:67]\n"
    "ds_read_b128 v[100:103], v2 offset:36864\n"
    "ds_read_b64 v[140:141], v147 offset:3072\n"
    "v_mfma_f32_16x16x32_bf16 v[72:75], v[244:247], v[108:111], v[72:75]\n"
    "ds_read_b64 v[142:143], v147 offset:3584\n"
    "global_load_dwordx4 a[108:111], v61, s[16:17] offset:-1024\n"
    "v_mfma_f32_16x16x32_bf16 v[68:71], v[104:107], v[252:255], v[68:71]\n"
    "s_waitcnt vmcnt(12)\n"
    "s_barrier\n"
    "v_mov_b32 v62, s98\n"
    "s_add_u32 s83, s47, 0x1f16080\n"
    "s_mov_b64 exec, s[68:69]\n"
    "buffer_store_dword v62, off, s[16:19], s83 sc1\n"
    "s_mov_b64 exec, -1\n"
    "v_mfma_f32_16x16x32_bf16 v[76:79], v[248:251], v[108:111], v[76:79]\n"
    "s_waitcnt lgkmcnt(4)\n"
    "v_add_f32 v128, v128, v136\n"
    "v_add_f32 v129, v129, v137\n"
    "v_mfma_f32_16x16x32_bf16 v[80:83], v[108:111], v[252:255], v[80:83]\n"
    "ds_read_b128 v[104:107], v63 offset:5120\n"
    "ds_read_b128 v[108:111], v2 offset:37888\n"
    "v_add_f32 v130, v130, v138\n"
    "v_mfma_f32_16x16x32_bf16 v[64:67], a[4:7], v[112:115], v[64:67]\n"
    "v_add_f32 v131, v131, v139\n"
    "s_waitcnt lgkmcnt(2)\n"
    "v_add_f32 v132, v132, v140\n"
    "v_mfma_f32_16x16x32_bf16 v[72:75], a[0:3], v[116:119], v[72:75]\n"
    "v_add_f32 v133, v133, v141\n"
    "v_add_f32 v134, v134, v142\n"
    "v_mfma_f32_16x16x32_bf16 v[68:71], v[112:115], a[8:11], v[68:71]\n"
    "v_add_f32 v135, v135, v143\n"
    "v_add_f32 v128, v128, v132\n"
    "v_mfma_f32_16x16x32_bf16 v[76:79], a[4:7], v[116:119], v[76:79]\n"
    "v_add_f32 v129, v129, v133\n"
    "v_add_f32 v130, v130, v134\n"
    "v_mfma_f32_16x16x32_bf16 v[80:83], v[116:119], a[8:11], v[80:83]\n"
    "ds_read_b128 v[112:115], v63 offset:5632\n"
    "ds_read_b128 v[116:119], v2 offset:38912\n"
    "v_mfma_f32_16x16x32_bf16 v[64:67], a[16:19], v[96:99], v[64:67]\n"
    "v_add_f32 v131, v131, v135\n"
    "v_add_f32 v128, v128, v130\n"
    "v_mfma_f32_16x16x32_bf16 v[72:75], a[12:15], v[100:103], v[72:75]\n"
    "v_add_f32 v129, v129, v131\n"
    "global_load_dwordx4 a[112:115], v61, s[16:17]\n"
    "v_mfma_f32_16x16x32_bf16 v[68:71], v[96:99], a[20:23], v[68:71]\n"
    "v_permlane32_swap_b32 v128, v129\n"
    "v_mfma_f32_16x16x32_bf16 v[76:79], a[16:19], v[100:103], v[76:79]\n"
    "v_add_f32 v128, v128, v129\n"
    "v_mov_b32 v130, v128\n"
    "v_mfma_f32_16x16x32_bf16 v[80:83], v[100:103], a[20:23], v[80:83]\n"
    "ds_read_b128 v[96:99], v63 offset:6144\n"
    "s_waitcnt lgkmcnt(3)\n"
    "v_mfma_f32_16x16x32_bf16 v[64:67], a[28:31], v[104:107], v[64:67]\n"
    "ds_read_b128 v[100:103], v2 offset:39936\n"
    "global_load_dwordx4 a[116:119], v61, s[16:17] offset:1024\n"
    "v_mfma_f32_16x16x32_bf16 v[72:75], a[24:27], v[108:111], v[72:75]\n"
    "v_permlane16_swap_b32 v128, v130\n"
    "v_mfma_f32_16x16x32_bf16 v[68:71], v[104:107], a[32:35], v[68:71]\n"
    "v_add_f32 v128, v128, v130\n"
    "v_mov_b32 v129, v128\n"
    "v_mfma_f32_16x16x32_bf16 v[76:79], a[28:31], v[108:111], v[76:79]\n"
    "v_permlane32_swap_b32 v128, v129\n"
    "v_mfma_f32_16x16x32_bf16 v[80:83], v[108:111], a[32:35], v[80:83]\n"
    "ds_read_b128 v[104:107], v63 offset:6656\n"
    "s_waitcnt lgkmcnt(3)\n"
    "v_mfma_f32_16x16x32_bf16 v[64:67], a[40:43], v[112:115], v[64:67]\n"
    "ds_read_b128 v[108:111], v2 offset:40960\n"
    "v_mul_f32 v131, s79, v128\n"
    "v_mfma_f32_16x16x32_bf16 v[72:75], a[36:39], v[116:119], v[72:75]\n"
    "v_mov_b32 v132, s81\n"
    "v_fma_f32 v132, v129, s80, v132\n"
    "v_mfma_f32_16x16x32_bf16 v[68:71], v[112:115], a[44:47], v[68:71]\n"
    "v_fma_f32 v132, -v131, v131, v132\n"
    "v_max_f32 v132, s81, v132\n"
    "v_mfma_f32_16x16x32_bf16 v[76:79], a[40:43], v[116:119], v[76:79]\n"
    "v_rsq_f32 v133, v132\n"
    "v_mfma_f32_16x16x32_bf16 v[80:83], v[116:119], a[44:47], v[80:83]\n"
    "ds_read_b128 v[112:115], v63 offset:7168\n"
    "s_waitcnt lgkmcnt(3)\n"
    "v_mfma_f32_16x16x32_bf16 v[64:67], a[52:55], v[96:99], v[64:67]\n"
    "ds_read_b128 v[116:119], v2 offset:41984\n"
    "v_cvt_pk_bf16_f32 v136, v131, v131\n"
    "v_mfma_f32_16x16x32_bf16 v[72:75], a[48:51], v[100:103], v[72:75]\n"
    "v_lshlrev_b32 v134, 16, v136\n"
    "v_sub_f32 v134, v131, v134\n"
    "v_mfma_f32_16x16x32_bf16 v[68:71], v[96:99], a[56:59], v[68:71]\n"
    "v_cvt_pk_bf16_f32 v137, v134, 0\n"
    "v_add_u32 v147, s72, v150\n"
    "v_mfma_f32_16x16x32_bf16 v[76:79], a[52:55], v[100:103], v[76:79]\n"
    "ds_write_b64 v147, v[136:137] offset:0\n"
    "v_mfma_f32_16x16x32_bf16 v[80:83], v[100:103], a[56:59], v[80:83]\n"
    "ds_read_b128 v[96:99], v63 offset:7680\n"
    "s_waitcnt vmcnt(9)\n"
    "s_waitcnt lgkmcnt(4)\n"
    "v_mfma_f32_16x16x32_bf16 v[64:67], a[64:67], v[104:107], v[64:67]\n"
    "ds_read_b128 v[100:103], v2 offset:43008\n"
    "v_add_u32 v147, s74, v14\n"
    "v_mfma_f32_16x16x32_bf16 v[72:75], a[60:63], v[108:111], v[72:75]\n"
    "ds_write_b32 v147, v133 offset:1536\n"
    "v_mfma_f32_16x16x32_bf16 v[68:71], v[104:107], a[68:71], v[68:71]\n"
    "global_load_dwordx4 a[120:123], v61, s[16:17] offset:2048\n"
    "v_mfma_f32_16x16x32_bf16 v[76:79], a[64:67], v[108:111], v[76:79]\n"
    "global_load_dwordx4 a[124:127], v61, s[16:17] offset:3072\n"
    "v_mfma_f32_16x16x32_bf16 v[80:83], v[108:111], a[68:71], v[80:83]\n"
    "s_waitcnt lgkmcnt(0)\n"
    "s_barrier\n"
    "v_mfma_f32_16x16x32_bf16 v[64:67], a[76:79], v[112:115], v[64:67]\n"
    "ds_read_b64 v[124:125], v15 offset:1024\n"
    "ds_read_b64 v[120:121], v15 offset:0\n"
    "v_mfma_f32_16x16x32_bf16 v[72:75], a[72:75], v[116:119], v[72:75]\n"
    "ds_read_b64 v[122:123], v15 offset:512\n"
    "ds_read_b128 v[104:107], v3 offset:1024\n"
    "v_mfma_f32_16x16x32_bf16 v[68:71], v[112:115], a[80:83], v[68:71]\n"
    "ds_read_b128 v[108:111], v3 offset:2048\n"
    "ds_read_b32 v144, v14 offset:1536\n"
    "v_mfma_f32_16x16x32_bf16 v[76:79], a[76:79], v[116:119], v[76:79]\n"
    "ds_read_b32 v145, v14 offset:1600\n"
    "ds_read_b32 v146, v14 offset:1664\n"
    "v_mfma_f32_16x16x32_bf16 v[80:83], v[116:119], a[80:83], v[80:83]\n"
    "ds_read_b128 v[112:115], v13 offset:1536\n"
    "s_waitcnt vmcnt(8)\n"
    "v_mfma_f32_16x16x32_bf16 v[64:67], a[88:91], v[96:99], v[64:67]\n"
    "ds_read_b128 v[116:119], v13 offset:1600\n"
    "v_add_u32 v61, 0x2000, v61\n"
    "global_load_dwordx4 a[128:131], v61, s[16:17] offset:-4096\n"
    "s_mov_b32 s82, 0x1f16080\n"
    "s_mov_b64 exec, 0xffff\n"
    "buffer_load_dword v60, v11, s[16:19], s82 offen sc1\n"
    "s_mov_b64 exec, -1\n"
    "v_mfma_f32_16x16x32_bf16 v[72:75], a[84:87], v[100:103], v[72:75]\n"
    "global_load_dwordx4 a[132:135], v61, s[16:17] offset:-3072\n"
    "v_mfma_f32_16x16x32_bf16 v[68:71], v[96:99], a[92:95], v[68:71]\n"
    "global_load_dwordx4 a[136:139], v61, s[16:17] offset:-2048\n"
    "v_mfma_f32_16x16x32_bf16 v[76:79], a[88:91], v[100:103], v[76:79]\n"
    "v_mfma_f32_16x16x32_bf16 v[80:83], v[100:103], a[92:95], v[80:83]\n"
    "ds_read_b128 v[96:99], v3 offset:3072\n"
    "s_waitcnt lgkmcnt(2)\n"
    "v_mfma_f32_16x16x16_bf16 v[84:87], v[48:49], v[124:125], 0\n"
    "ds_read_b128 v[100:103], v3 offset:4096\n"
    "v_mfma_f32_16x16x16_bf16 v[88:91], v[50:51], v[124:125], 0\n"
    "global_load_dwordx4 a[140:143], v61, s[16:17] offset:-1024\n"
    "v_mfma_f32_16x16x16_bf16 v[92:95], v[124:125], v[52:53], 0\n"
    "v_mfma_f32_16x16x16_bf16 v[64:67], v[50:51], v[120:121], v[64:67]\n"
    "v_mfma_f32_16x16x16_bf16 v[72:75], v[48:49], v[122:123], v[72:75]\n"
    "v_mfma_f32_16x16x16_bf16 v[76:79], v[50:51], v[122:123], v[76:79]\n"
    "global_load_dwordx4 a[144:147], v61, s[16:17]\n"
    "v_mfma_f32_16x16x16_bf16 v[68:71], v[120:121], v[52:53], v[68:71]\n"
    "v_mfma_f32_16x16x16_bf16 v[80:83], v[122:123], v[52:53], v[80:83]\n"
    "v_mul_f32 v64, v64, v144\n"
    "v_mul_f32 v65, v65, v144\n"
    "v_mfma_f32_16x16x32_bf16 v[84:87], v[160:163], v[104:107], v[84:87]\n"
    "v_mul_f32 v66, v66, v144\n"
    "v_mul_f32 v67, v67, v144\n"
    "v_mfma_f32_16x16x32_bf16 v[88:91], v[164:167], v[104:107], v[88:91]\n"
    "v_cvt_pk_bf16_f32 v126, v64, v65\n"
    "v_cvt_pk_bf16_f32 v127, v66, v67\n"
    "v_mfma_f32_16x16x32_bf16 v[92:95], v[104:107], v[168:171], v[92:95]\n"
    "ds_read_b128 v[104:107], v3 offset:5120\n"
    "v_cvt_pk_bf16_f32 v132, v72, v73\n"
    "v_mfma_f32_16x16x32_bf16 v[84:87], v[172:175], v[108:111], v[84:87]\n"
    "v_cvt_pk_bf16_f32 v133, v74, v75\n"
    "v_mul_f32 v76, v76, v145\n"
    "v_mfma_f32_16x16x32_bf16 v[88:91], v[176:179], v[108:111], v[88:91]\n"
    "v_mul_f32 v77, v77, v145\n"
    "v_mul_f32 v78, v78, v145\n"
    "v_mfma_f32_16x16x32_bf16 v[92:95], v[108:111], v[180:183], v[92:95]\n"
    "ds_read_b128 v[108:111], v3 offset:6144\n"
    "s_waitcnt lgkmcnt(2)\n"
    "v_mfma_f32_16x16x32_bf16 v[84:87], v[184:187], v[96:99], v[84:87]\n"
    "v_mul_f32 v79, v79, v145\n"
    "v_cvt_pk_bf16_f32 v128, v76, v77\n"
    "v_mfma_f32_16x16x32_bf16 v[88:91], v[188:191], v[96:99], v[88:91]\n"
    "v_cvt_pk_bf16_f32 v129, v78, v79\n"
    "v_mul_f32 v68, v68, v112\n"
    "v_mfma_f32_16x16x32_bf16 v[92:95], v[96:99], v[192:195], v[92:95]\n"
    "ds_read_b128 v[96:99], v3 offset:7168\n"
    "v_mul_f32 v69, v69, v113\n"
    "v_mfma_f32_16x16x32_bf16 v[84:87], v[196:199], v[100:103], v[84:87]\n"
    "v_mul_f32 v70, v70, v114\n"
    "v_mul_f32 v71, v71, v115\n"
    "v_mfma_f32_16x16x32_bf16 v[88:91], v[200:203], v[100:103], v[88:91]\n"
    "v_cvt_pk_bf16_f32 v120, v68, v69\n"
    "v_cvt_pk_bf16_f32 v121, v70, v71\n"
    "v_mfma_f32_16x16x32_bf16 v[92:95], v[100:103], v[204:207], v[92:95]\n"
    "ds_read_b128 v[100:103], v3 offset:8192\n"
    "ds_read_b128 v[112:115], v13 offset:1664\n"
    "v_mul_f32 v80, v80, v116\n"
    "s_waitcnt lgkmcnt(4)\n"
    "v_mfma_f32_16x16x32_bf16 v[84:87], v[208:211], v[104:107], v[84:87]\n"
    "v_mul_f32 v81, v81, v117\n"
    "v_mul_f32 v82, v82, v118\n"
    "v_mfma_f32_16x16x32_bf16 v[88:91], v[212:215], v[104:107], v[88:91]\n"
    "v_mul_f32 v83, v83, v119\n"
    "v_cvt_pk_bf16_f32 v122, v80, v81\n"
    "v_mfma_f32_16x16x32_bf16 v[92:95], v[104:107], v[216:219], v[92:95]\n"
    "ds_read_b128 v[104:107], v3 offset:9216\n"
    "v_cvt_pk_bf16_f32 v123, v82, v83\n"
    "s_waitcnt lgkmcnt(4)\n"
    "v_mfma_f32_16x16x32_bf16 v[84:87], v[220:223], v[108:111], v[84:87]\n"
    "v_mfma_f32_16x16x16_bf16 v[64:67], v[126:127], v[132:133], v[32:35]\n"
    "v_mfma_f32_16x16x16_bf16 v[68:71], v[128:129], v[132:133], v[40:43]\n"
    "v_mul_f32 v142, s76, v145\n"
    "global_load_dwordx4 a[148:151], v61, s[16:17] offset:1024\n"
    "v_mfma_f32_16x16x32_bf16 v[88:91], v[224:227], v[108:111], v[88:91]\n"
    "v_max3_f32 v140, v64, v65, v66\n"
    "s_nop 2\n"
    "v_max3_f32 v141, v67, v68, v69\n"
    "v_mfma_f32_16x16x32_bf16 v[92:95], v[108:111], v[228:231], v[92:95]\n"
    "ds_read_b128 v[108:111], v3 offset:10240\n"
    "s_waitcnt lgkmcnt(2)\n"
    "v_mfma_f32_16x16x32_bf16 v[84:87], v[232:235], v[96:99], v[84:87]\n"
    "v_max3_f32 v140, v140, v70, v71\n"
    "v_max_f32 v140, v140, v141\n"
    "v_mfma_f32_16x16x32_bf16 v[88:91], v[236:239], v[96:99], v[88:91]\n"
    "v_mov_b32 v141, v140\n"
    "global_load_dwordx4 a[152:155], v61, s[16:17] offset:2048\n"
    "v_mfma_f32_16x16x32_bf16 v[92:95], v[96:99], v[240:243], v[92:95]\n"
    "ds_read_b128 v[96:99], v3 offset:11264\n"
    "v_permlane16_swap_b32 v141, v140\n"
    "v_mfma_f32_16x16x32_bf16 v[84:87], v[244:247], v[100:103], v[84:87]\n"
    "v_max_f32 v140, v140, v141\n"
    "v_mov_b32 v141, v140\n"
    "v_mfma_f32_16x16x32_bf16 v[88:91], v[248:251], v[100:103], v[88:91]\n"
    "v_permlane32_swap_b32 v141, v140\n"
    "v_mfma_f32_16x16x32_bf16 v[92:95], v[100:103], v[252:255], v[92:95]\n"
    "ds_read_b128 v[100:103], v3 offset:12288\n"
    "s_waitcnt lgkmcnt(3)\n"
    "v_mfma_f32_16x16x32_bf16 v[84:87], a[0:3], v[104:107], v[84:87]\n"
    "v_max_f32 v140, v140, v141\n"
    "v_mul_f32 v140, v142, v140\n"
    "v_mfma_f32_16x16x32_bf16 v[88:91], a[4:7], v[104:107], v[88:91]\n"
    "v_fma_f32 v64, v64, v142, -v140\n"
    "v_fma_f32 v65, v65, v142, -v140\n"
    "v_mfma_f32_16x16x32_bf16 v[92:95], v[104:107], a[8:11], v[92:95]\n"
    "ds_read_b128 v[104:107], v3 offset:13312\n"
    "v_fma_f32 v66, v66, v142, -v140\n"
    "s_waitcnt lgkmcnt(3)\n"
    "v_mfma_f32_16x16x32_bf16 v[84:87], a[12:15], v[108:111], v[84:87]\n"
    "v_fma_f32 v67, v67, v142, -v140\n"
    "v_fma_f32 v68, v68, v142, -v140\n"
    "v_mfma_f32_16x16x32_bf16 v[88:91], a[16:19], v[108:111], v[88:91]\n"
    "v_fma_f32 v69, v69, v142, -v140\n"
    "v_fma_f32 v70, v70, v142, -v140\n"
    "v_mfma_f32_16x16x32_bf16 v[92:95], v[108:111], a[20:23], v[92:95]\n"
    "ds_read_b128 v[108:111], v3 offset:14336\n"
    "v_fma_f32 v71, v71, v142, -v140\n"
    "v_exp_f32 v64, v64\n"
    "s_waitcnt lgkmcnt(2)\n"
    "v_mfma_f32_16x16x32_bf16 v[84:87], a[24:27], v[96:99], v[84:87]\n"
    "v_exp_f32 v65, v65\n"
    "v_mfma_f32_16x16x32_bf16 v[88:91], a[28:31], v[96:99], v[88:91]\n"
    "v_exp_f32 v66, v66\n"
    "v_mfma_f32_16x16x32_bf16 v[92:95], v[96:99], a[32:35], v[92:95]\n"
    "ds_read_b128 v[96:99], v3 offset:15360\n"
    "global_load_dwordx4 a[156:159], v61, s[16:17] offset:3072\n"
    "v_mfma_f32_16x16x32_bf16 v[84:87], a[36:39], v[100:103], v[84:87]\n"
    "v_exp_f32 v67, v67\n"
    "v_mfma_f32_16x16x32_bf16 v[88:91], a[40:43], v[100:103], v[88:91]\n"
    "v_exp_f32 v68, v68\n"
    "v_mfma_f32_16x16x32_bf16 v[92:95], v[100:103], a[44:47], v[92:95]\n"
    "ds_read_b128 v[100:103], v3 offset:16384\n"
    "v_exp_f32 v69, v69\n"
    "s_waitcnt lgkmcnt(2)\n"
    "v_mfma_f32_16x16x32_bf16 v[84:87], a[48:51], v[104:107], v[84:87]\n"
    "v_exp_f32 v70, v70\n"
    "v_mfma_f32_16x16x32_bf16 v[88:91], a[52:55], v[104:107], v[88:91]\n"
    "v_exp_f32 v71, v71\n"
    "v_mfma_f32_16x16x32_bf16 v[92:95], v[104:107], a[56:59], v[92:95]\n"
    "v_cvt_pk_bf16_f32 v72, v64, v65\n"
    "v_cvt_pk_bf16_f32 v73, v66, v67\n"
    "v_mfma_f32_16x16x32_bf16 v[84:87], a[60:63], v[108:111], v[84:87]\n"
    "s_mov_b64 exec, 0xffff\n"
    "s_waitcnt vmcnt(7)\n"
    "v_cmp_ne_u32 vcc, s98, v60\n"
    "s_mov_b64 exec, -1\n"
    "s_cbranch_vccz .Lpd8\n"
    "s_mov_b64 exec, 65535\n"
    ".Lps8:\n"
    "s_sleep 1\n"
    "buffer_load_dword v60, v11, s[16:19], s82 offen sc1\n"
    "s_waitcnt vmcnt(0)\n"
    "v_cmp_ne_u32 vcc, s98, v60\n"
    "s_nop 1\n"
    "s_cbranch_vccz .Lpe8\n"
    "s_sub_u32 s67, s67, 1\n"
    "s_cbranch_scc0 .Lps8\n"
    "s_mov_b32 s67, 0\n"
    ".Lpe8:\n"
    "s_mov_b64 exec, -1\n"
    ".Lpd8:\n"
    "s_add_u32 m0, s48, 0x16000\n"
    "s_add_u32 s87, s95, 0x1188000\n"
    "v_add_u32 v62, s87, v148\n"
    "global_load_lds_dwordx4 v62, s[16:17] sc1\n"
    "s_add_u32 m0, s49, 0x1a000\n"
    "s_add_u32 s87, s31, 0x1188000\n"
    "v_add_u32 v62, s87, v1\n"
    "global_load_lds_dwordx4 v62, s[16:17] sc1\n"
    "global_load_lds_dwordx4 v62, s[16:17] offset:1024 sc1\n"
    "v_mfma_f32_16x16x32_bf16 v[88:91], a[64:67], v[108:111], v[88:91]\n"
    "v_cvt_pk_bf16_f32 v74, v68, v69\n"
    "v_cvt_pk_bf16_f32 v75, v70, v71\n"
    "v_mfma_f32_16x16x32_bf16 v[92:95], v[108:111], a[68:71], v[92:95]\n"
    "v_mfma_f32_16x16x32_bf16 v[80:83], v[44:47], v[72:75], 0\n"
    "s_add_u32 m0, s48, 0x17000\n"
    "s_add_u32 s87, s95, 0x118a000\n"
    "v_add_u32 v62, s87, v148\n"
    "global_load_lds_dwordx4 v62, s[16:17] sc1\n"
    "s_waitcnt lgkmcnt(0)\n"
    "v_mfma_f32_16x16x32_bf16 v[84:87], a[72:75], v[96:99], v[84:87]\n"
    "v_mfma_f32_16x16x32_bf16 v[76:79], v[120:123], v[72:75], 0\n"
    "v_rcp_f32 v141, v80\n"
    "v_mfma_f32_16x16x32_bf16 v[88:91], a[76:79], v[96:99], v[88:91]\n"
    "s_add_u32 m0, s49, 0x1c000\n"
    "s_add_u32 s87, s31, 0x118a000\n"
    "v_add_u32 v62, s87, v1\n"
    "global_load_lds_dwordx4 v62, s[16:17] sc1\n"
    "global_load_lds_dwordx4 v62, s[16:17] offset:1024 sc1\n"
    "v_mfma_f32_16x16x32_bf16 v[92:95], v[96:99], a[80:83], v[92:95]\n"
    "v_mul_f32 v76, v76, v141\n"
    "v_mul_f32 v77, v77, v141\n"
    "v_mfma_f32_16x16x32_bf16 v[84:87], a[84:87], v[100:103], v[84:87]\n"
    "v_mul_f32 v78, v78, v141\n"
    "v_mul_f32 v79, v79, v141\n"
    "v_mfma_f32_16x16x32_bf16 v[88:91], a[88:91], v[100:103], v[88:91]\n"
    "v_cvt_pk_bf16_f32 v136, v76, v77\n"
    "v_cvt_pk_bf16_f32 v137, v78, v79\n"
    "v_mfma_f32_16x16x32_bf16 v[92:95], v[100:103], a[92:95], v[92:95]\n"
    "s_nop 3\n"
    "v_mul_f32 v88, v88, v146\n"
    "v_mul_f32 v89, v89, v146\n"
    "v_mul_f32 v90, v90, v146\n"
    "v_mul_f32 v91, v91, v146\n"
    "v_cvt_pk_bf16_f32 v130, v88, v89\n"
    "v_cvt_pk_bf16_f32 v131, v90, v91\n"
    "v_cvt_pk_bf16_f32 v134, v84, v85\n"
    "v_cvt_pk_bf16_f32 v135, v86, v87\n"
    "s_nop 1\n"
    "v_mfma_f32_16x16x16_bf16 v[64:67], v[128:129], v[134:135], v[36:39]\n"
    "s_add_u32 m0, s48, 0x22000\n"
    "s_add_u32 s87, s34, 0x1382000\n"
    "v_add_u32 v62, s87, v1\n"
    "global_load_lds_dwordx4 v62, s[16:17] sc1\n"
    "v_mfma_f32_16x16x16_bf16 v[68:71], v[130:131], v[134:135], v[40:43]\n"
    "v_mul_f32 v142, s76, v146\n"
    "v_mul_f32 v92, v92, v112\n"
    "v_mul_f32 v93, v93, v113\n"
    "v_mul_f32 v94, v94, v114\n"
    "v_mul_f32 v95, v95, v115\n"
    "v_cvt_pk_bf16_f32 v124, v92, v93\n"
    "v_cvt_pk_bf16_f32 v125, v94, v95\n"
    "v_max3_f32 v140, v64, v65, v66\n"
    "v_max3_f32 v141, v67, v68, v69\n"
    "v_max3_f32 v140, v140, v70, v71\n"
    "v_max_f32 v140, v140, v141\n"
    "s_add_u32 m0, s49, 0x23000\n"
    "s_add_u32 s87, s35, 0x1382000\n"
    "v_add_u32 v62, s87, v1\n"
    "global_load_lds_dwordx4 v62, s[16:17] sc1\n"
    "global_load_lds_dwordx4 v62, s[16:17] offset:1024 sc1\n"
    "v_mov_b32 v141, v140\n"
    "s_nop 1\n"
    "v_permlane16_swap_b32 v141, v140\n"
    "v_max_f32 v140, v140, v141\n"
    "v_mov_b32 v141, v140\n"
    "s_nop 1\n"
    "v_permlane32_swap_b32 v141, v140\n"
    "v_max_f32 v140, v140, v141\n"
    "v_mul_f32 v140, v142, v140\n"
    "v_fma_f32 v64, v64, v142, -v140\n"
    "v_fma_f32 v65, v65, v142, -v140\n"
    "v_fma_f32 v66, v66, v142, -v140\n"
    "v_fma_f32 v67, v67, v142, -v140\n"
    "v_fma_f32 v68, v68, v142, -v140\n"
    "v_fma_f32 v69, v69, v142, -v140\n"
    "v_fma_f32 v70, v70, v142, -v140\n"
    "v_fma_f32 v71, v71, v142, -v140\n"
    "v_exp_f32 v64, v64\n"
    "v_exp_f32 v65, v65\n"
    "v_exp_f32 v66, v66\n"
    "v_exp_f32 v67, v67\n"
    "s_add_u32 m0, s50, 0x1e000\n"
    "s_add_u32 s87, s96, 0x1188000\n"
    "v_add_u32 v62, s87, v1\n"
    "global_load_lds_dwordx4 v62, s[16:17] sc1\n"
    "global_load_lds_dwordx4 v62, s[16:17] offset:1024 sc1\n"
    "global_load_lds_dwordx4 v62, s[16:17] offset:2048 sc1\n"
    "global_load_lds_dwordx4 v62, s[16:17] offset:3072 sc1\n"
    "v_exp_f32 v68, v68\n"
    "v_exp_f32 v69, v69\n"
    "v_exp_f32 v70, v70\n"
    "v_exp_f32 v71, v71\n"
    "v_cvt_pk_bf16_f32 v72, v64, v65\n"
    "v_cvt_pk_bf16_f32 v73, v66, v67\n"
    "v_cvt_pk_bf16_f32 v74, v68, v69\n"
    "v_cvt_pk_bf16_f32 v75, v70, v71\n"
    "s_nop 1\n"
    "v_mfma_f32_16x16x32_bf16 v[80:83], v[44:47], v[72:75], 0\n"
    "v_mfma_f32_16x16x32_bf16 v[76:79], v[122:125], v[72:75], 0\n"
    "s_nop 3\n"
    "v_rcp_f32 v141, v80\n"
    "s_nop 2\n"
    "v_mul_f32 v76, v76, v141\n"
    "v_mul_f32 v77, v77, v141\n"
    "v_mul_f32 v78, v78, v141\n"
    "v_mul_f32 v79, v79, v141\n"
    "v_cvt_pk_bf16_f32 v138, v76, v77\n"
    "v_cvt_pk_bf16_f32 v139, v78, v79\n"
    "s_nop 0\n"
    "v_permlane16_swap_b32 v136, v138\n"
    "v_permlane16_swap_b32 v137, v139\n"
    "s_add_u32 s84, s44, 0x1400000\n"
    "buffer_store_dwordx4 v[136:139], v1, s[16:19], s84 offen sc1\n"
    "v_add_u32 v61, s40, v1\n"
    "global_load_dwordx4 a[160:163], v61, s[16:17] offset:-4096\n"
    "global_load_dwordx4 a[164:167], v61, s[16:17] offset:-3072\n"
    "global_load_dwordx4 a[168:171], v61, s[16:17] offset:-2048\n"
    "s_waitcnt vmcnt(14)\n"
    "s_barrier\n"
    "v_add_u32 v63, 0x16000, v149\n"
    "ds_read_b128 v[96:99], v63 offset:0\n"
    "ds_read_b128 v[100:103], v4 offset:8192\n"
    "ds_read_b128 v[104:107], v63 offset:512\n"
    "ds_read_b128 v[108:111], v4 offset:9216\n"
    "ds_read_b128 v[112:115], v63 offset:1024\n"
    "ds_read_b128 v[116:119], v4 offset:10240\n"
    "global_load_dwordx4 a[172:175], v61, s[16:17] offset:-1024\n"
    "s_waitcnt lgkmcnt(4)\n"
    "v_mfma_f32_16x16x32_bf16 v[64:67], v[164:167], v[96:99], 0\n"
    "global_load_dwordx4 a[176:179], v61, s[16:17]\n"
    "v_mfma_f32_16x16x32_bf16 v[72:75], v[160:163], v[100:103], 0\n"
    "v_mfma_f32_16x16x32_bf16 v[68:71], v[96:99], v[168:171], 0\n"
    "global_load_dwordx4 a[180:183], v61, s[16:17] offset:1024\n"
    "v_mfma_f32_16x16x32_bf16 v[76:79], v[164:167], v[100:103], 0\n"
    "v_mfma_f32_16x16x32_bf16 v[80:83], v[100:103], v[168:171], 0\n"
    "ds_read_b128 v[96:99], v63 offset:1536\n"
    "s_waitcnt lgkmcnt(1)\n"
    "v_mfma_f32_16x16x32_bf16 v[64:67], v[176:179], v[104:107], v[64:67]\n"
    "ds_read_b128 v[100:103], v4 offset:11264\n"
    "v_mfma_f32_16x16x32_bf16 v[72:75], v[172:175], v[108:111], v[72:75]\n"
    "global_load_dwordx4 a[184:187], v61, s[16:17] offset:2048\n"
    "v_mfma_f32_16x16x32_bf16 v[68:71], v[104:107], v[180:183], v[68:71]\n"
    "v_mfma_f32_16x16x32_bf16 v[76:79], v[176:179], v[108:111], v[76:79]\n"
    "v_mfma_f32_16x16x32_bf16 v[80:83], v[108:111], v[180:183], v[80:83]\n"
    "ds_read_b128 v[104:107], v63 offset:2048\n"
    "ds_read_b128 v[108:111], v4 offset:12288\n"
    "v_mfma_f32_16x16x32_bf16 v[64:67], v[188:191], v[112:115], v[64:67]\n"
    "global_load_dwordx4 a[188:191], v61, s[16:17] offset:3072\n"
    "v_mfma_f32_16x16x32_bf16 v[72:75], v[184:187], v[116:119], v[72:75]\n"
    "v_mfma_f32_16x16x32_bf16 v[68:71], v[112:115], v[192:195], v[68:71]\n"
    "v_mfma_f32_16x16x32_bf16 v[76:79], v[188:191], v[116:119], v[76:79]\n"
    "v_mfma_f32_16x16x32_bf16 v[80:83], v[116:119], v[192:195], v[80:83]\n"
    "ds_read_b128 v[112:115], v63 offset:2560\n"
    "s_waitcnt lgkmcnt(3)\n"
    "v_mfma_f32_16x16x32_bf16 v[64:67], v[200:203], v[96:99], v[64:67]\n"
    "ds_read_b128 v[116:119], v4 offset:13312\n"
    "v_add_u32 v61, 0x2000, v61\n"
    "global_load_dwordx4 a[192:195], v61, s[16:17] offset:-4096\n"
    "v_mfma_f32_16x16x32_bf16 v[72:75], v[196:199], v[100:103], v[72:75]\n"
    "v_mfma_f32_16x16x32_bf16 v[68:71], v[96:99], v[204:207], v[68:71]\n"
    "global_load_dwordx4 a[196:199], v61, s[16:17] offset:-3072\n"
    "v_mfma_f32_16x16x32_bf16 v[76:79], v[200:203], v[100:103], v[76:79]\n"
    "v_mfma_f32_16x16x32_bf16 v[80:83], v[100:103], v[204:207], v[80:83]\n"
    "ds_read_b128 v[96:99], v63 offset:3072\n"
    "s_waitcnt lgkmcnt(3)\n"
    "v_mfma_f32_16x16x32_bf16 v[64:67], v[212:215], v[104:107], v[64:67]\n"
    "ds_read_b128 v[100:103], v4 offset:14336\n"
    "v_mfma_f32_16x16x32_bf16 v[72:75], v[208:211], v[108:111], v[72:75]\n"
    "global_load_dwordx4 a[200:203], v61, s[16:17] offset:-2048\n"
    "v_mfma_f32_16x16x32_bf16 v[68:71], v[104:107], v[216:219], v[68:71]\n"
    "v_mfma_f32_16x16x32_bf16 v[76:79], v[212:215], v[108:111], v[76:79]\n"
    "v_mfma_f32_16x16x32_bf16 v[80:83], v[108:111], v[216:219], v[80:83]\n"
    "ds_read_b128 v[104:107], v63 offset:3584\n"
    "s_waitcnt lgkmcnt(3)\n"
    "v_mfma_f32_16x16x32_bf16 v[64:67], v[224:227], v[112:115], v[64:67]\n"
    "ds_read_b128 v[108:111], v4 offset:15360\n"
    "global_load_dwordx4 a[204:207], v61, s[16:17] offset:-1024\n"
    "v_mfma_f32_16x16x32_bf16 v[72:75], v[220:223], v[116:119], v[72:75]\n"
    "v_mfma_f32_16x16x32_bf16 v[68:71], v[112:115], v[228:231], v[68:71]\n"
    "v_mfma_f32_16x16x32_bf16 v[76:79], v[224:227], v[116:119], v[76:79]\n"
    "v_mfma_f32_16x16x32_bf16 v[80:83], v[116:119], v[228:231], v[80:83]\n"
    "s_waitcnt vmcnt(15)\n"
    "s_barrier\n"
    "s_waitcnt lgkmcnt(2)\n"
    "v_mfma_f32_16x16x32_bf16 v[64:67], v[236:239], v[96:99], v[64:67]\n"
    "ds_read_b128 v[112:115], v63 offset:4096\n"
    "ds_read_b128 v[116:119], v4 offset:16384\n"
    "s_add_u32 s63, s70, 0x22000\n"
    "v_add_u32 v147, s63, v12\n"
    "v_mfma_f32_16x16x32_bf16 v[72:75], v[232:235], v[100:103], v[72:75]\n"
    "ds_read_b64 v[128:129], v147 offset:0\n"
    "ds_read_b64 v[130:131], v147 offset:512\n"
    "v_mfma_f32_16x16x32_bf16 v[68:71], v[96:99], v[240:243], v[68:71]\n"
    "ds_read_b64 v[132:133], v147 offset:1024\n"
    "ds_read_b64 v[134:135], v147 offset:1536\n"
    "v_mfma_f32_16x16x32_bf16 v[76:79], v[236:239], v[100:103], v[76:79]\n"
    "ds_read_b64 v[136:137], v147 offset:2048\n"
    "ds_read_b64 v[138:139], v147 offset:2560\n"
    "v_mfma_f32_16x16x32_bf16 v[80:83], v[100:103], v[240:243], v[80:83]\n"
    "ds_read_b128 v[96:99], v63 offset:4608\n"
    "s_waitcnt lgkmcnt(9)\n"
    "v_mfma_f32_16x16x32_bf16 v[64:67], v[248:251], v[104:107], v[64:67]\n"
    "s_waitcnt vmcnt(12)\n"
    "s_barrier\n"
    "v_mov_b32 v62, s98\n"
    "s_add_u32 s83, s47, 0x1f18000\n"
    "s_mov_b64 exec, s[68:69]\n"
    "buffer_store_dword v62, off, s[16:19], s83 sc1\n"
    "s_mov_b64 exec, -1\n"
    "v_mfma_f32_16x16x32_bf16 v[72:75], v[244:247], v[108:111], v[72:75]\n"
    "ds_read_b128 v[100:103], v4 offset:17408\n"
    "ds_read_b64 v[140:141], v147 offset:3072\n"
    "v_mfma_f32_16x16x32_bf16 v[68:71], v[104:107], v[252:255], v[68:71]\n"
    "ds_read_b64 v[142:143], v147 offset:3584\n"
    "global_load_dwordx4 a[208:211], v61, s[16:17]\n"
    "v_mfma_f32_16x16x32_bf16 v[76:79], v[248:251], v[108:111], v[76:79]\n"
    "s_waitcnt lgkmcnt(4)\n"
    "v_add_f32 v128, v128, v136\n"
    "v_add_f32 v129, v129, v137\n"
    "v_mfma_f32_16x16x32_bf16 v[80:83], v[108:111], v[252:255], v[80:83]\n"
    "ds_read_b128 v[104:107], v63 offset:5120\n"
    "ds_read_b128 v[108:111], v4 offset:18432\n"
    "v_add_f32 v130, v130, v138\n"
    "v_mfma_f32_16x16x32_bf16 v[64:67], a[4:7], v[112:115], v[64:67]\n"
    "v_add_f32 v131, v131, v139\n"
    "global_load_dwordx4 a[212:215], v61, s[16:17] offset:1024\n"
    "v_mfma_f32_16x16x32_bf16 v[72:75], a[0:3], v[116:119], v[72:75]\n"
    "global_load_dwordx4 a[220:223], v61, s[16:17] offset:2048\n"
    "v_mfma_f32_16x16x32_bf16 v[68:71], v[112:115], a[8:11], v[68:71]\n"
    "s_waitcnt lgkmcnt(3)\n"
    "v_add_f32 v132, v132, v140\n"
    "v_add_f32 v133, v133, v141\n"
    "s_waitcnt lgkmcnt(2)\n"
    "v_add_f32 v134, v134, v142\n"
    "v_mfma_f32_16x16x32_bf16 v[76:79], a[4:7], v[116:119], v[76:79]\n"
    "v_add_f32 v135, v135, v143\n"
    "v_add_f32 v128, v128, v132\n"
    "v_mfma_f32_16x16x32_bf16 v[80:83], v[116:119], a[8:11], v[80:83]\n"
    "ds_read_b128 v[112:115], v63 offset:5632\n"
    "ds_read_b128 v[116:119], v4 offset:19456\n"
    "v_mfma_f32_16x16x32_bf16 v[64:67], a[16:19], v[96:99], v[64:67]\n"
    "v_add_f32 v129, v129, v133\n"
    "v_add_f32 v130, v130, v134\n"
    "v_mfma_f32_16x16x32_bf16 v[72:75], a[12:15], v[100:103], v[72:75]\n"
    "v_add_f32 v131, v131, v135\n"
    "v_add_f32 v128, v128, v130\n"
    "v_mfma_f32_16x16x32_bf16 v[68:71], v[96:99], a[20:23], v[68:71]\n"
    "v_add_f32 v129, v129, v131\n"
    "global_load_dwordx4 a[236:239], v61, s[16:17] offset:3072\n"
    "v_mfma_f32_16x16x32_bf16 v[76:79], a[16:19], v[100:103], v[76:79]\n"
    "v_permlane32_swap_b32 v128, v129\n"
    "v_mfma_f32_16x16x32_bf16 v[80:83], v[100:103], a[20:23], v[80:83]\n"
    "ds_read_b128 v[96:99], v63 offset:6144\n"
    "s_waitcnt lgkmcnt(3)\n"
    "v_mfma_f32_16x16x32_bf16 v[64:67], a[28:31], v[104:107], v[64:67]\n"
    "ds_read_b128 v[100:103], v4 offset:20480\n"
    "v_add_f32 v128, v128, v129\n"
    "v_mfma_f32_16x16x32_bf16 v[72:75], a[24:27], v[108:111], v[72:75]\n"
    "v_mov_b32 v130, v128\n"
    "v_add_u32 v61, 0x2000, v61\n"
    "global_load_dwordx4 a[252:255], v61, s[16:17] offset:-4096\n"
    "v_mfma_f32_16x16x32_bf16 v[68:71], v[104:107], a[32:35], v[68:71]\n"
    "v_permlane16_swap_b32 v128, v130\n"
    "v_mfma_f32_16x16x32_bf16 v[76:79], a[28:31], v[108:111], v[76:79]\n"
    "v_add_f32 v128, v128, v130\n"
    "v_mov_b32 v129, v128\n"
    "v_mfma_f32_16x16x32_bf16 v[80:83], v[108:111], a[32:35], v[80:83]\n"
    "ds_read_b128 v[104:107], v63 offset:6656\n"
    "s_waitcnt lgkmcnt(3)\n"
    "v_mfma_f32_16x16x32_bf16 v[64:67], a[40:43], v[112:115], v[64:67]\n"
    "ds_read_b128 v[108:111], v4 offset:21504\n"
    "v_permlane32_swap_b32 v128, v129\n"
    "v_mfma_f32_16x16x32_bf16 v[72:75], a[36:39], v[116:119], v[72:75]\n"
    "v_mul_f32 v131, s79, v128\n"
    "v_mov_b32 v132, s81\n"
    "v_mfma_f32_16x16x32_bf16 v[68:71], v[112:115], a[44:47], v[68:71]\n"
    "v_fma_f32 v132, v129, s80, v132\n"
    "v_fma_f32 v132, -v131, v131, v132\n"
    "v_mfma_f32_16x16x32_bf16 v[76:79], a[40:43], v[116:119], v[76:79]\n"
    "v_max_f32 v132, s81, v132\n"
    "global_load_dwordx4 a[232:235], v61, s[16:17] offset:-3072\n"
    "v_mfma_f32_16x16x32_bf16 v[80:83], v[116:119], a[44:47], v[80:83]\n"
    "ds_read_b128 v[112:115], v63 offset:7168\n"
    "s_waitcnt lgkmcnt(3)\n"
    "v_mfma_f32_16x16x32_bf16 v[64:67], a[52:55], v[96:99], v[64:67]\n"
    "ds_read_b128 v[116:119], v4 offset:22528\n"
    "global_load_dwordx4 a[248:251], v61, s[16:17] offset:-2048\n"
    "v_mfma_f32_16x16x32_bf16 v[72:75], a[48:51], v[100:103], v[72:75]\n"
    "v_rsq_f32 v133, v132\n"
    "v_mfma_f32_16x16x32_bf16 v[68:71], v[96:99], a[56:59], v[68:71]\n"
    "v_cvt_pk_bf16_f32 v136, v131, v131\n"
    "v_lshlrev_b32 v134, 16, v136\n"
    "v_mfma_f32_16x16x32_bf16 v[76:79], a[52:55], v[100:103], v[76:79]\n"
    "v_sub_f32 v134, v131, v134\n"
    "v_cvt_pk_bf16_f32 v137, v134, 0\n"
    "v_mfma_f32_16x16x32_bf16 v[80:83], v[100:103], a[56:59], v[80:83]\n"
    "ds_read_b128 v[96:99], v63 offset:7680\n"
    "s_waitcnt lgkmcnt(3)\n"
    "v_mfma_f32_16x16x32_bf16 v[64:67], a[64:67], v[104:107], v[64:67]\n"
    "ds_read_b128 v[100:103], v4 offset:23552\n"
    "v_add_u32 v147, s72, v150\n"
    "v_mfma_f32_16x16x32_bf16 v[72:75], a[60:63], v[108:111], v[72:75]\n"
    "ds_write_b64 v147, v[136:137] offset:2048\n"
    "v_mfma_f32_16x16x32_bf16 v[68:71], v[104:107], a[68:71], v[68:71]\n"
    "v_add_u32 v147, s74, v14\n"
    "global_load_dwordx4 v[156:159], v61, s[16:17] offset:-1024\n"
    "v_mfma_f32_16x16x32_bf16 v[76:79], a[64:67], v[108:111], v[76:79]\n"
    "ds_write_b32 v147, v133 offset:3584\n"
    "v_mfma_f32_16x16x32_bf16 v[80:83], v[108:111], a[68:71], v[80:83]\n"
    "s_waitcnt lgkmcnt(0)\n"
    "s_barrier\n"
    "v_mfma_f32_16x16x32_bf16 v[64:67], a[76:79], v[112:115], v[64:67]\n"
    "ds_read_b64 v[124:125], v15 offset:3072\n"
    "ds_read_b64 v[120:121], v15 offset:2048\n"
    "v_mfma_f32_16x16x32_bf16 v[72:75], a[72:75], v[116:119], v[72:75]\n"
    "ds_read_b64 v[122:123], v15 offset:2560\n"
    "ds_read_b128 v[104:107], v4 offset:24576\n"
    "s_mov_b32 s82, 0x1f18000\n"
    "s_mov_b64 exec, 0xff\n"
    "buffer_load_dword v60, v11, s[16:19], s82 offen sc1\n"
    "s_mov_b64 exec, -1\n"
    "v_mfma_f32_16x16x32_bf16 v[68:71], v[112:115], a[80:83], v[68:71]\n"
    "ds_read_b128 v[108:111], v4 offset:25600\n"
    "ds_read_b32 v144, v14 offset:3584\n"
    "v_mfma_f32_16x16x32_bf16 v[76:79], a[76:79], v[116:119], v[76:79]\n"
    "ds_read_b32 v145, v14 offset:3648\n"
    "ds_read_b32 v146, v14 offset:3712\n"
    "v_mfma_f32_16x16x32_bf16 v[80:83], v[116:119], a[80:83], v[80:83]\n"
    "ds_read_b128 v[112:115], v13 offset:3584\n"
    "ds_read_b128 v[116:119], v13 offset:3648\n"
    "v_mfma_f32_16x16x32_bf16 v[64:67], a[88:91], v[96:99], v[64:67]\n"
    "global_load_dwordx4 a[216:219], v61, s[16:17]\n"
    "v_mfma_f32_16x16x32_bf16 v[72:75], a[84:87], v[100:103], v[72:75]\n"
    "global_load_dwordx4 a[228:231], v61, s[16:17] offset:1024\n"
    "v_mfma_f32_16x16x32_bf16 v[68:71], v[96:99], a[92:95], v[68:71]\n"
    "global_load_dwordx4 a[244:247], v61, s[16:17] offset:2048\n"
    "v_mfma_f32_16x16x32_bf16 v[76:79], a[88:91], v[100:103], v[76:79]\n"
    "v_mfma_f32_16x16x32_bf16 v[80:83], v[100:103], a[92:95], v[80:83]\n"
    "ds_read_b128 v[96:99], v4 offset:26624\n"
    "s_waitcnt lgkmcnt(5)\n"
    "v_mfma_f32_16x16x16_bf16 v[84:87], v[48:49], v[124:125], 0\n"
    "ds_read_b128 v[100:103], v4 offset:27648\n"
    "global_load_dwordx4 a[224:227], v61, s[16:17] offset:3072\n"
    "v_mfma_f32_16x16x16_bf16 v[88:91], v[50:51], v[124:125], 0\n"
    "v_mfma_f32_16x16x16_bf16 v[92:95], v[124:125], v[52:53], 0\n"
    "v_add_u32 v61, 0x2000, v61\n"
    "global_load_dwordx4 a[240:243], v61, s[16:17] offset:-4096\n"
    "v_mfma_f32_16x16x16_bf16 v[64:67], v[50:51], v[120:121], v[64:67]\n"
    "v_mfma_f32_16x16x16_bf16 v[72:75], v[48:49], v[122:123], v[72:75]\n"
    "v_mfma_f32_16x16x16_bf16 v[76:79], v[50:51], v[122:123], v[76:79]\n"
    "v_mfma_f32_16x16x16_bf16 v[68:71], v[120:121], v[52:53], v[68:71]\n"
    "global_load_dwordx4 v[152:155], v61, s[16:17] offset:-3072\n"
    "v_mfma_f32_16x16x16_bf16 v[80:83], v[122:123], v[52:53], v[80:83]\n"
    "v_mul_f32 v64, v64, v144\n"
    "v_mul_f32 v65, v65, v144\n"
    "v_mfma_f32_16x16x32_bf16 v[84:87], v[160:163], v[104:107], v[84:87]\n"
    "v_mul_f32 v66, v66, v144\n"
    "v_mul_f32 v67, v67, v144\n"
    "v_mfma_f32_16x16x32_bf16 v[88:91], v[164:167], v[104:107], v[88:91]\n"
    "v_cvt_pk_bf16_f32 v126, v64, v65\n"
    "v_cvt_pk_bf16_f32 v127, v66, v67\n"
    "v_mfma_f32_16x16x32_bf16 v[92:95], v[104:107], v[168:171], v[92:95]\n"
    "ds_read_b128 v[104:107], v4 offset:28672\n"
    "v_cvt_pk_bf16_f32 v132, v72, v73\n"
    "v_mfma_f32_16x16x32_bf16 v[84:87], v[172:175], v[108:111], v[84:87]\n"
    "v_cvt_pk_bf16_f32 v133, v74, v75\n"
    "s_waitcnt lgkmcnt(1)\n"
    "v_mul_f32 v76, v76, v145\n"
    "v_mfma_f32_16x16x32_bf16 v[88:91], v[176:179], v[108:111], v[88:91]\n"
    "v_mul_f32 v77, v77, v145\n"
    "v_mul_f32 v78, v78, v145\n"
    "v_mfma_f32_16x16x32_bf16 v[92:95], v[108:111], v[180:183], v[92:95]\n"
    "ds_read_b128 v[108:111], v4 offset:29696\n"
    "v_mul_f32 v79, v79, v145\n"
    "v_mfma_f32_16x16x32_bf16 v[84:87], v[184:187], v[96:99], v[84:87]\n"
    "v_cvt_pk_bf16_f32 v128, v76, v77\n"
    "v_cvt_pk_bf16_f32 v129, v78, v79\n"
    "v_mfma_f32_16x16x32_bf16 v[88:91], v[188:191], v[96:99], v[88:91]\n"
    "v_mul_f32 v68, v68, v112\n"
    "v_mul_f32 v69, v69, v113\n"
    "v_mfma_f32_16x16x32_bf16 v[92:95], v[96:99], v[192:195], v[92:95]\n"
    "ds_read_b128 v[96:99], v4 offset:30720\n"
    "v_mul_f32 v70, v70, v114\n"
    "v_mfma_f32_16x16x32_bf16 v[84:87], v[196:199], v[100:103], v[84:87]\n"
    "v_mul_f32 v71, v71, v115\n"
    "v_cvt_pk_bf16_f32 v120, v68, v69\n"
    "v_mfma_f32_16x16x32_bf16 v[88:91], v[200:203], v[100:103], v[88:91]\n"
    "v_cvt_pk_bf16_f32 v121, v70, v71\n"
    "ds_read_b128 v[112:115], v13 offset:3712\n"
    "v_mfma_f32_16x16x32_bf16 v[92:95], v[100:103], v[204:207], v[92:95]\n"
    "ds_read_b128 v[100:103], v4 offset:31744\n"
    "v_mul_f32 v80, v80, v116\n"
    "s_waitcnt lgkmcnt(4)\n"
    "v_mfma_f32_16x16x32_bf16 v[84:87], v[208:211], v[104:107], v[84:87]\n"
    "v_mul_f32 v81, v81, v117\n"
    "v_mul_f32 v82, v82, v118\n"
    "v_mfma_f32_16x16x32_bf16 v[88:91], v[212:215], v[104:107], v[88:91]\n"
    "v_mul_f32 v83, v83, v119\n"
    "v_cvt_pk_bf16_f32 v122, v80, v81\n"
    "v_mfma_f32_16x16x32_bf16 v[92:95], v[104:107], v[216:219], v[92:95]\n"
    "ds_read_b128 v[104:107], v4 offset:32768\n"
    "v_cvt_pk_bf16_f32 v123, v82, v83\n"
    "s_waitcnt lgkmcnt(4)\n"
    "v_mfma_f32_16x16x32_bf16 v[84:87], v[220:223], v[108:111], v[84:87]\n"
    "v_mfma_f32_16x16x16_bf16 v[64:67], v[126:127], v[132:133], v[32:35]\n"
    "v_mfma_f32_16x16x16_bf16 v[68:71], v[128:129], v[132:133], v[40:43]\n"
    "v_mul_f32 v142, s76, v145\n"
    "global_load_dwordx4 v[160:163], v61, s[16:17] offset:-2048\n"
    "v_mfma_f32_16x16x32_bf16 v[88:91], v[224:227], v[108:111], v[88:91]\n"
    "v_max3_f32 v140, v64, v65, v66\n"
    "s_nop 2\n"
    "v_max3_f32 v141, v67, v68, v69\n"
    "v_mfma_f32_16x16x32_bf16 v[92:95], v[108:111], v[228:231], v[92:95]\n"
    "ds_read_b128 v[108:111], v4 offset:33792\n"
    "s_waitcnt lgkmcnt(2)\n"
    "v_mfma_f32_16x16x32_bf16 v[84:87], v[232:235], v[96:99], v[84:87]\n"
    "v_max3_f32 v140, v140, v70, v71\n"
    "v_max_f32 v140, v140, v141\n"
    "v_mfma_f32_16x16x32_bf16 v[88:91], v[236:239], v[96:99], v[88:91]\n"
    "v_mov_b32 v141, v140\n"
    "global_load_dwordx4 v[164:167], v61, s[16:17] offset:-1024\n"
    "v_mfma_f32_16x16x32_bf16 v[92:95], v[96:99], v[240:243], v[92:95]\n"
    "ds_read_b128 v[96:99], v4 offset:34816\n"
    "v_permlane16_swap_b32 v141, v140\n"
    "v_mfma_f32_16x16x32_bf16 v[84:87], v[244:247], v[100:103], v[84:87]\n"
    "v_max_f32 v140, v140, v141\n"
    "v_mov_b32 v141, v140\n"
    "v_mfma_f32_16x16x32_bf16 v[88:91], v[248:251], v[100:103], v[88:91]\n"
    "v_permlane32_swap_b32 v141, v140\n"
    "v_mfma_f32_16x16x32_bf16 v[92:95], v[100:103], v[252:255], v[92:95]\n"
    "ds_read_b128 v[100:103], v4 offset:35840\n"
    "s_waitcnt lgkmcnt(3)\n"
    "v_mfma_f32_16x16x32_bf16 v[84:87], a[0:3], v[104:107], v[84:87]\n"
    "v_max_f32 v140, v140, v141\n"
    "v_mul_f32 v140, v142, v140\n"
    "v_mfma_f32_16x16x32_bf16 v[88:91], a[4:7], v[104:107], v[88:91]\n"
    "v_fma_f32 v64, v64, v142, -v140\n"
    "v_fma_f32 v65, v65, v142, -v140\n"
    "v_mfma_f32_16x16x32_bf16 v[92:95], v[104:107], a[8:11], v[92:95]\n"
    "ds_read_b128 v[104:107], v4 offset:36864\n"
    "v_fma_f32 v66, v66, v142, -v140\n"
    "s_waitcnt lgkmcnt(3)\n"
    "v_mfma_f32_16x16x32_bf16 v[84:87], a[12:15], v[108:111], v[84:87]\n"
    "v_fma_f32 v67, v67, v142, -v140\n"
    "v_fma_f32 v68, v68, v142, -v140\n"
    "v_mfma_f32_16x16x32_bf16 v[88:91], a[16:19], v[108:111], v[88:91]\n"
    "v_fma_f32 v69, v69, v142, -v140\n"
    "v_fma_f32 v70, v70, v142, -v140\n"
    "v_mfma_f32_16x16x32_bf16 v[92:95], v[108:111], a[20:23], v[92:95]\n"
    "ds_read_b128 v[108:111], v4 offset:37888\n"
    "v_fma_f32 v71, v71, v142, -v140\n"
    "v_exp_f32 v64, v64\n"
    "s_waitcnt lgkmcnt(2)\n"
    "v_mfma_f32_16x16x32_bf16 v[84:87], a[24:27], v[96:99], v[84:87]\n"
    "v_exp_f32 v65, v65\n"
    "v_mfma_f32_16x16x32_bf16 v[88:91], a[28:31], v[96:99], v[88:91]\n"
    "v_exp_f32 v66, v66\n"
    "s_mov_b64 exec, 0xff\n"
    "s_waitcnt vmcnt(8)\n"
    "v_cmp_ne_u32 vcc, s98, v60\n"
    "s_mov_b64 exec, -1\n"
    "s_cbranch_vccz .Lpd9\n"
    "s_mov_b64 exec, 255\n"
    ".Lps9:\n"
    "s_sleep 1\n"
    "buffer_load_dword v60, v11, s[16:19], s82 offen sc1\n"
    "s_waitcnt vmcnt(0)\n"
    "v_cmp_ne_u32 vcc, s98, v60\n"
    "s_nop 1\n"
    "s_cbranch_vccz .Lpe9\n"
    "s_sub_u32 s67, s67, 1\n"
    "s_cbranch_scc0 .Lps9\n"
    "s_mov_b32 s67, 0\n"
    ".Lpe9:\n"
    "s_mov_b64 exec, -1\n"
    ".Lpd9:\n"
    "s_add_u32 m0, s51, 0\n"
    "s_add_u32 s87, s36, 0x1400000\n"
    "v_add_u32 v62, s87, v1\n"
    "global_load_lds_dwordx4 v62, s[16:17] sc1\n"
    "global_load_lds_dwordx4 v62, s[16:17] offset:1024 sc1\n"
    "global_load_lds_dwordx4 v62, s[16:17] offset:2048 sc1\n"
    "global_load_lds_dwordx4 v62, s[16:17] offset:3072 sc1\n"
    "v_mfma_f32_16x16x32_bf16 v[92:95], v[96:99], a[32:35], v[92:95]\n"
    "ds_read_b128 v[96:99], v4 offset:38912\n"
    "v_mfma_f32_16x16x32_bf16 v[84:87], a[36:39], v[100:103], v[84:87]\n"
    "v_exp_f32 v67, v67\n"
    "v_mfma_f32_16x16x32_bf16 v[88:91], a[40:43], v[100:103], v[88:91]\n"
    "v_exp_f32 v68, v68\n"
    "v_mfma_f32_16x16x32_bf16 v[92:95], v[100:103], a[44:47], v[92:95]\n"
    "ds_read_b128 v[100:103], v4 offset:39936\n"
    "v_exp_f32 v69, v69\n"
    "s_waitcnt lgkmcnt(2)\n"
    "v_mfma_f32_16x16x32_bf16 v[84:87], a[48:51], v[104:107], v[84:87]\n"
    "v_exp_f32 v70, v70\n"
    "v_mfma_f32_16x16x32_bf16 v[88:91], a[52:55], v[104:107], v[88:91]\n"
    "v_exp_f32 v71, v71\n"
    "v_mfma_f32_16x16x32_bf16 v[92:95], v[104:107], a[56:59], v[92:95]\n"
    "s_add_u32 m0, s51, 0x1000\n"
    "s_add_u32 s87, s36, 0x1401000\n"
    "v_add_u32 v62, s87, v1\n"
    "global_load_lds_dwordx4 v62, s[16:17] sc1\n"
    "global_load_lds_dwordx4 v62, s[16:17] offset:1024 sc1\n"
    "global_load_lds_dwordx4 v62, s[16:17] offset:2048 sc1\n"
    "global_load_lds_dwordx4 v62, s[16:17] offset:3072 sc1\n"
    "v_cvt_pk_bf16_f32 v72, v64, v65\n"
    "v_mfma_f32_16x16x32_bf16 v[84:87], a[60:63], v[108:111], v[84:87]\n"
    "v_cvt_pk_bf16_f32 v73, v66, v67\n"
    "v_cvt_pk_bf16_f32 v74, v68, v69\n"
    "v_mfma_f32_16x16x32_bf16 v[88:91], a[64:67], v[108:111], v[88:91]\n"
    "v_cvt_pk_bf16_f32 v75, v70, v71\n"
    "v_mfma_f32_16x16x32_bf16 v[92:95], v[108:111], a[68:71], v[92:95]\n"
    "v_mfma_f32_16x16x32_bf16 v[80:83], v[44:47], v[72:75], 0\n"
    "s_waitcnt lgkmcnt(0)\n"
    "v_mfma_f32_16x16x32_bf16 v[84:87], a[72:75], v[96:99], v[84:87]\n"
    "v_mfma_f32_16x16x32_bf16 v[76:79], v[120:123], v[72:75], 0\n"
    "v_rcp_f32 v141, v80\n"
    "v_mfma_f32_16x16x32_bf16 v[88:91], a[76:79], v[96:99], v[88:91]\n"
    "v_mfma_f32_16x16x32_bf16 v[92:95], v[96:99], a[80:83], v[92:95]\n"
    "v_mul_f32 v76, v76, v141\n"
    "v_mul_f32 v77, v77, v141\n"
    "v_mfma_f32_16x16x32_bf16 v[84:87], a[84:87], v[100:103], v[84:87]\n"
    "v_mul_f32 v78, v78, v141\n"
    "v_mul_f32 v79, v79, v141\n"
    "v_mfma_f32_16x16x32_bf16 v[88:91], a[88:91], v[100:103], v[88:91]\n"
    "v_cvt_pk_bf16_f32 v136, v76, v77\n"
    "v_cvt_pk_bf16_f32 v137, v78, v79\n"
    "v_mfma_f32_16x16x32_bf16 v[92:95], v[100:103], a[92:95], v[92:95]\n"
    "s_nop 3\n"
    "v_mul_f32 v88, v88, v146\n"
    "v_mul_f32 v89, v89, v146\n"
    "v_mul_f32 v90, v90, v146\n"
    "v_mul_f32 v91, v91, v146\n"
    "v_cvt_pk_bf16_f32 v130, v88, v89\n"
    "v_cvt_pk_bf16_f32 v131, v90, v91\n"
    "v_cvt_pk_bf16_f32 v134, v84, v85\n"
    "v_cvt_pk_bf16_f32 v135, v86, v87\n"
    "s_nop 1\n"
    "v_mfma_f32_16x16x16_bf16 v[64:67], v[128:129], v[134:135], v[36:39]\n"
    "v_mfma_f32_16x16x16_bf16 v[68:71], v[130:131], v[134:135], v[40:43]\n"
    "v_mul_f32 v142, s76, v146\n"
    "v_mul_f32 v92, v92, v112\n"
    "v_mul_f32 v93, v93, v113\n"
    "v_mul_f32 v94, v94, v114\n"
    "v_mul_f32 v95, v95, v115\n"
    "v_cvt_pk_bf16_f32 v124, v92, v93\n"
    "v_cvt_pk_bf16_f32 v125, v94, v95\n"
    "v_max3_f32 v140, v64, v65, v66\n"
    "v_max3_f32 v141, v67, v68, v69\n"
    "v_max3_f32 v140, v140, v70, v71\n"
    "v_max_f32 v140, v140, v141\n"
    "v_mov_b32 v141, v140\n"
    "global_load_dwordx4 v[168:171], v61, s[16:17]\n"
    "s_nop 0\n"
    "v_permlane16_swap_b32 v141, v140\n"
    "v_max_f32 v140, v140, v141\n"
    "v_mov_b32 v141, v140\n"
    "global_load_dwordx4 v[172:175], v61, s[16:17] offset:1024\n"
    "s_nop 0\n"
    "v_permlane32_swap_b32 v141, v140\n"
    "v_max_f32 v140, v140, v141\n"
    "v_mul_f32 v140, v142, v140\n"
    "v_fma_f32 v64, v64, v142, -v140\n"
    "v_fma_f32 v65, v65, v142, -v140\n"
    "v_fma_f32 v66, v66, v142, -v140\n"
    "v_fma_f32 v67, v67, v142, -v140\n"
    "v_fma_f32 v68, v68, v142, -v140\n"
    "v_fma_f32 v69, v69, v142, -v140\n"
    "v_fma_f32 v70, v70, v142, -v140\n"
    "v_fma_f32 v71, v71, v142, -v140\n"
    "v_exp_f32 v64, v64\n"
    "v_exp_f32 v65, v65\n"
    "v_exp_f32 v66, v66\n"
    "v_exp_f32 v67, v67\n"
    "v_exp_f32 v68, v68\n"
    "v_exp_f32 v69, v69\n"
    "v_exp_f32 v70, v70\n"
    "v_exp_f32 v71, v71\n"
    "v_cvt_pk_bf16_f32 v72, v64, v65\n"
    "v_cvt_pk_bf16_f32 v73, v66, v67\n"
    "v_cvt_pk_bf16_f32 v74, v68, v69\n"
    "v_cvt_pk_bf16_f32 v75, v70, v71\n"
    "global_load_dwordx4 v[176:179], v61, s[16:17] offset:2048\n"
    "s_nop 0\n"
    "v_mfma_f32_16x16x32_bf16 v[80:83], v[44:47], v[72:75], 0\n"
    "global_load_dwordx4 v[180:183], v61, s[16:17] offset:3072\n"
    "v_mfma_f32_16x16x32_bf16 v[76:79], v[122:125], v[72:75], 0\n"
    "s_nop 3\n"
    "v_rcp_f32 v141, v80\n"
    "s_nop 2\n"
    "v_mul_f32 v76, v76, v141\n"
    "v_mul_f32 v77, v77, v141\n"
    "v_mul_f32 v78, v78, v141\n"
    "v_mul_f32 v79, v79, v141\n"
    "v_cvt_pk_bf16_f32 v138, v76, v77\n"
    "v_cvt_pk_bf16_f32 v139, v78, v79\n"
    "v_add_u32 v61, 0x2000, v61\n"
    "global_load_dwordx4 v[184:187], v61, s[16:17] offset:-4096\n"
    "v_permlane16_swap_b32 v136, v138\n"
    "v_permlane16_swap_b32 v137, v139\n"
    "s_add_u32 s84, s44, 0x1408000\n"
    "buffer_store_dwordx4 v[136:139], v1, s[16:19], s84 offen sc1\n"
    "global_load_dwordx4 v[188:191], v61, s[16:17] offset:-3072\n"
    "global_load_dwordx4 v[192:195], v61, s[16:17] offset:-2048\n"
    "global_load_dwordx4 v[196:199], v61, s[16:17] offset:-1024\n"
    "global_load_dwordx4 v[200:203], v61, s[16:17]\n"
    "s_waitcnt vmcnt(14)\n"
    "s_add_u32 s63, s51, 0\n"
    "v_add_u32 v5, s63, v1\n"
    "ds_read_b128 v[96:99], v5 offset:0\n"
    "ds_read_b128 v[100:103], v5 offset:1024\n"
    "ds_read_b128 v[104:107], v5 offset:2048\n"
    "ds_read_b128 v[108:111], v5 offset:3072\n"
    "s_add_u32 s64, s54, 0x1000\n"
    "v_add_u32 v120, s64, v1\n"
    "s_add_u32 s64, s55, 0\n"
    "v_add_u32 v121, s64, v1\n"
    "s_add_u32 s64, s56, 0x800\n"
    "v_add_u32 v122, s64, v1\n"
    "global_load_dwordx4 v[204:207], v61, s[16:17] offset:1024\n"
    "global_load_dwordx4 v[208:211], v61, s[16:17] offset:2048\n"
    "s_waitcnt lgkmcnt(2)\n"
    "v_mfma_f32_32x32x16_bf16 v[64:79], a[96:99], v[96:99], 0\n"
    "global_load_dwordx4 v[212:215], v61, s[16:17] offset:3072\n"
    "v_mfma_f32_32x32x16_bf16 v[80:95], a[100:103], v[96:99], 0\n"
    "v_add_u32 v61, 0x2000, v61\n"
    "global_load_dwordx4 v[216:219], v61, s[16:17] offset:-4096\n"
    "v_mfma_f32_32x32x16_bf16 v[64:79], a[104:107], v[100:103], v[64:79]\n"
    "v_mfma_f32_32x32x16_bf16 v[80:95], a[108:111], v[100:103], v[80:95]\n"
    "s_waitcnt lgkmcnt(0)\n"
    "v_mfma_f32_32x32x16_bf16 v[64:79], a[112:115], v[104:107], v[64:79]\n"
    "global_load_dwordx4 v[220:223], v61, s[16:17] offset:-3072\n"
    "s_waitcnt vmcnt(15)\n"
    "ds_read_b128 v[112:115], v5 offset:4096\n"
    "v_mfma_f32_32x32x16_bf16 v[80:95], a[116:119], v[104:107], v[80:95]\n"
    "ds_read_b128 v[116:119], v5 offset:5120\n"
    "ds_read_b128 v[96:99], v5 offset:6144\n"
    "ds_read_b128 v[100:103], v5 offset:7168\n"
    "global_load_dwordx4 v[224:227], v61, s[16:17] offset:-2048\n"
    "v_mfma_f32_32x32x16_bf16 v[64:79], a[120:123], v[108:111], v[64:79]\n"
    "v_mfma_f32_32x32x16_bf16 v[80:95], a[124:127], v[108:111], v[80:95]\n"
    "global_load_dwordx4 v[228:231], v61, s[16:17] offset:-1024\n"
    "s_waitcnt lgkmcnt(3)\n"
    "v_mfma_f32_32x32x16_bf16 v[80:95], a[132:135], v[112:115], v[80:95]\n"
    "s_waitcnt lgkmcnt(1)\n"
    "v_mfma_f32_32x32x16_bf16 v[80:95], a[140:143], v[116:119], v[80:95]\n"
    "global_load_dwordx4 v[232:235], v61, s[16:17]\n"
    "v_mfma_f32_32x32x16_bf16 v[80:95], a[148:151], v[96:99], v[80:95]\n"
    "s_waitcnt lgkmcnt(0)\n"
    "v_mfma_f32_32x32x16_bf16 v[80:95], a[156:159], v[100:103], v[80:95]\n"
    "global_load_dwordx4 v[236:239], v61, s[16:17] offset:1024\n"
    "v_mfma_f32_32x32x16_bf16 v[64:79], a[128:131], v[112:115], v[64:79]\n"
    "s_nop 3\n"
    "ds_write_b128 v5, v[80:83] offset:0\n"
    "v_mfma_f32_32x32x16_bf16 v[64:79], a[136:139], v[116:119], v[64:79]\n"
    "global_load_dwordx4 v[240:243], v61, s[16:17] offset:2048\n"
    "v_mfma_f32_32x32x16_bf16 v[64:79], a[144:147], v[96:99], v[64:79]\n"
    "ds_write_b128 v5, v[84:87] offset:1024\n"
    "s_waitcnt vmcnt(14)\n"
    "s_barrier\n"
    "v_mov_b32 v62, s98\n"
    "s_add_u32 s83, s47, 0x1f18080\n"
    "s_mov_b64 exec, s[68:69]\n"
    "buffer_store_dword v62, off, s[16:19], s83 sc1\n"
    "s_mov_b64 exec, -1\n"
    "ds_write_b128 v5, v[88:91] offset:2048\n"
    "v_mfma_f32_32x32x16_bf16 v[64:79], a[152:155], v[100:103], v[64:79]\n"
    "ds_write_b128 v5, v[92:95] offset:3072\n"
    "global_load_dwordx4 v[244:247], v61, s[16:17] offset:3072\n"
    "s_nop 7\n"
    "s_nop 1\n"
    "ds_write_b128 v5, v[72:75] offset:4096\n"
    "ds_write_b128 v5, v[76:79] offset:5120\n"
    "v_add_f32 v16, v16, v64\n"
    "v_add_f32 v17, v17, v65\n"
    "v_add_f32 v18, v18, v66\n"
    "v_add_f32 v19, v19, v67\n"
    "v_add_f32 v20, v20, v68\n"
    "v_add_f32 v21, v21, v69\n"
    "v_add_f32 v22, v22, v70\n"
    "v_add_f32 v23, v23, v71\n"
    "s_waitcnt lgkmcnt(0)\n"
    "s_barrier\n"
    "ds_read_b128 v[104:107], v121 offset:0\n"
    "ds_read_b128 v[108:111], v121 offset:1024\n"
    "ds_read_b128 v[112:115], v122 offset:0\n"
    "ds_read_b128 v[116:119], v122 offset:1024\n"
    "ds_read_b128 v[96:99], v120 offset:0\n"
    "ds_read_b128 v[100:103], v120 offset:1024\n"
    "v_add_u32 v61, s41, v1\n"
    "global_load_dwordx4 v[248:251], v61, s[16:17] offset:-4096\n"
    "global_load_dwordx4 v[252:255], v61, s[16:17] offset:-3072\n"
    "global_load_dwordx4 a[0:3], v61, s[16:17] offset:-2048\n"
    "global_load_dwordx4 a[4:7], v61, s[16:17] offset:-1024\n"
    "s_waitcnt lgkmcnt(2)\n"
    "v_add_f32 v104, v104, v112\n"
    "v_add_f32 v105, v105, v113\n"
    "v_add_f32 v106, v106, v114\n"
    "v_add_f32 v107, v107, v115\n"
    "v_add_f32 v108, v108, v116\n"
    "v_add_f32 v109, v109, v117\n"
    "v_add_f32 v110, v110, v118\n"
    "v_add_f32 v111, v111, v119\n"
    "s_waitcnt lgkmcnt(0)\n"
    "v_add_f32 v16, v16, v96\n"
    "v_add_f32 v17, v17, v97\n"
    "v_add_f32 v18, v18, v98\n"
    "v_add_f32 v19, v19, v99\n"
    "v_add_f32 v20, v20, v100\n"
    "v_add_f32 v21, v21, v101\n"
    "v_add_f32 v22, v22, v102\n"
    "v_add_f32 v23, v23, v103\n"
    "v_add_f32 v16, v16, v104\n"
    "v_add_f32 v17, v17, v105\n"
    "v_add_f32 v18, v18, v106\n"
    "v_add_f32 v19, v19, v107\n"
    "v_add_f32 v20, v20, v108\n"
    "v_add_f32 v21, v21, v109\n"
    "v_add_f32 v22, v22, v110\n"
    "v_add_f32 v23, v23, v111\n"
    "v_cvt_pk_bf16_f32 v124, v16, v17\n"
    "v_cvt_pk_bf16_f32 v125, v18, v19\n"
    "v_cvt_pk_bf16_f32 v126, v20, v21\n"
    "v_cvt_pk_bf16_f32 v127, v22, v23\n"
    "s_add_u32 s84, s42, 0x1600000\n"
    "buffer_store_dwordx4 v[124:127], v8, s[16:19], s84 offen sc1\n"
    "v_add_f32 v128, v16, v17\n"
    "v_mul_f32 v129, v16, v16\n"
    "v_add_f32 v130, v18, v19\n"
    "v_mul_f32 v131, v17, v17\n"
    "v_fma_f32 v129, v18, v18, v129\n"
    "v_fma_f32 v131, v19, v19, v131\n"
    "v_add_f32 v128, v128, v20\n"
    "v_add_f32 v130, v130, v21\n"
    "v_fma_f32 v129, v20, v20, v129\n"
    "v_fma_f32 v131, v21, v21, v131\n"
    "v_add_f32 v128, v128, v22\n"
    "v_add_f32 v130, v130, v23\n"
    "v_fma_f32 v129, v22, v22, v129\n"
    "v_fma_f32 v131, v23, v23, v131\n"
    "v_add_f32 v128, v128, v130\n"
    "v_add_f32 v129, v129, v131\n"
    "s_add_u32 s85, s43, 0x1800000\n"
    "global_load_dwordx4 a[8:11], v61, s[16:17]\n"
    "v_permlane32_swap_b32 v128, v129\n"
    "v_add_f32 v128, v128, v129\n"
    "buffer_store_dword v128, v9, s[16:19], s85 offen sc1\n"
    "global_load_dwordx4 a[12:15], v61, s[16:17] offset:1024\n"
    "global_load_dwordx4 a[16:19], v61, s[16:17] offset:2048\n"
    "s_mov_b32 s82, 0x1f18080\n"
    "s_mov_b64 exec, 0xff\n"
    "buffer_load_dword v60, v11, s[16:19], s82 offen sc1\n"
    "s_mov_b64 exec, -1\n"
    "global_load_dwordx4 a[20:23], v61, s[16:17] offset:3072\n"
    "v_add_u32 v61, 0x2000, v61\n"
    "global_load_dwordx4 a[24:27], v61, s[16:17] offset:-4096\n"
    "global_load_dwordx4 a[28:31], v61, s[16:17] offset:-3072\n"
    "global_load_dwordx4 a[32:35], v61, s[16:17] offset:-2048\n"
    "global_load_dwordx4 a[36:39], v61, s[16:17] offset:-1024\n"
    "global_load_dwordx4 a[40:43], v61, s[16:17]\n"
    "global_load_dwordx4 a[44:47], v61, s[16:17] offset:1024\n"
    "global_load_dwordx4 a[48:51], v61, s[16:17] offset:2048\n"
    "global_load_dwordx4 a[52:55], v61, s[16:17] offset:3072\n"
    "v_add_u32 v61, 0x2000, v61\n"
    "global_load_dwordx4 a[56:59], v61, s[16:17] offset:-4096\n"
    "global_load_dwordx4 a[60:63], v61, s[16:17] offset:-3072\n"
    "s_waitcnt vmcnt(14)\n"
    "s_barrier\n"
    "v_mov_b32 v62, s98\n"
    "s_add_u32 s83, s47, 0x1f1a000\n"
    "s_mov_b64 exec, s[68:69]\n"
    "buffer_store_dword v62, off, s[16:19], s83 sc1\n"
    "s_mov_b64 exec, -1\n"
    "global_load_dwordx4 a[64:67], v61, s[16:17] offset:-2048\n"
    "global_load_dwordx4 a[68:71], v61, s[16:17] offset:-1024\n"
    "global_load_dwordx4 a[72:75], v61, s[16:17]\n"
    "s_mov_b64 exec, 0xff\n"
    "s_waitcnt vmcnt(15)\n"
    "v_cmp_ne_u32 vcc, s98, v60\n"
    "s_mov_b64 exec, -1\n"
    "s_cbranch_vccz .Lpd10\n"
    "s_mov_b64 exec, 255\n"
    ".Lps10:\n"
    "s_sleep 1\n"
    "buffer_load_dword v60, v11, s[16:19], s82 offen sc1\n"
    "s_waitcnt vmcnt(0)\n"
    "v_cmp_ne_u32 vcc, s98, v60\n"
    "s_nop 1\n"
    "s_cbranch_vccz .Lpe10\n"
    "s_sub_u32 s67, s67, 1\n"
    "s_cbranch_scc0 .Lps10\n"
    "s_mov_b32 s67, 0\n"
    ".Lpe10:\n"
    "s_mov_b64 exec, -1\n"
    ".Lpd10:\n"
    "s_add_u32 m0, s51, 0x8000\n"
    "s_add_u32 s87, s36, 0x1408000\n"
    "v_add_u32 v62, s87, v1\n"
    "global_load_lds_dwordx4 v62, s[16:17] sc1\n"
    "global_load_lds_dwordx4 v62, s[16:17] offset:1024 sc1\n"
    "global_load_lds_dwordx4 v62, s[16:17] offset:2048 sc1\n"
    "global_load_lds_dwordx4 v62, s[16:17] offset:3072 sc1\n"
    "s_add_u32 m0, s51, 0x9000\n"
    "s_add_u32 s87, s36, 0x1409000\n"
    "v_add_u32 v62, s87, v1\n"
    "global_load_lds_dwordx4 v62, s[16:17] sc1\n"
    "global_load_lds_dwordx4 v62, s[16:17] offset:1024 sc1\n"
    "global_load_lds_dwordx4 v62, s[16:17] offset:2048 sc1\n"
    "global_load_lds_dwordx4 v62, s[16:17] offset:3072 sc1\n"
    "s_mov_b32 s82, 0x1f1a000\n"
    "s_mov_b64 exec, 0xff\n"
    "buffer_load_dword v60, v11, s[16:19], s82 offen sc1\n"
    "s_mov_b64 exec, -1\n"
    "global_load_dwordx4 a[76:79], v61, s[16:17] offset:1024\n"
    "global_load_dwordx4 a[80:83], v61, s[16:17] offset:2048\n"
    "global_load_dwordx4 a[84:87], v61, s[16:17] offset:3072\n"
    "v_add_u32 v61, 0x2000, v61\n"
    "global_load_dwordx4 a[88:91], v61, s[16:17] offset:-4096\n"
    "global_load_dwordx4 a[92:95], v61, s[16:17] offset:-3072\n"
    "s_waitcnt vmcnt(10)\n"
    "s_add_u32 s63, s51, 0x8000\n"
    "v_add_u32 v5, s63, v1\n"
    "ds_read_b128 v[96:99], v5 offset:0\n"
    "ds_read_b128 v[100:103], v5 offset:1024\n"
    "ds_read_b128 v[104:107], v5 offset:2048\n"
    "ds_read_b128 v[108:111], v5 offset:3072\n"
    "s_add_u32 s64, s54, 0x9000\n"
    "v_add_u32 v120, s64, v1\n"
    "s_add_u32 s64, s55, 0x8000\n"
    "v_add_u32 v121, s64, v1\n"
    "s_add_u32 s64, s56, 0x8800\n"
    "v_add_u32 v122, s64, v1\n"
    "s_waitcnt lgkmcnt(2)\n"
    "v_mfma_f32_32x32x16_bf16 v[64:79], a[96:99], v[96:99], 0\n"
    "global_load_dwordx4 a[96:99], v61, s[16:17] offset:-2048\n"
    "v_mfma_f32_32x32x16_bf16 v[80:95], a[100:103], v[96:99], 0\n"
    "global_load_dwordx4 a[100:103], v61, s[16:17] offset:-1024\n"
    "v_mfma_f32_32x32x16_bf16 v[64:79], a[104:107], v[100:103], v[64:79]\n"
    "global_load_dwordx4 a[104:107], v61, s[16:17]\n"
    "v_mfma_f32_32x32x16_bf16 v[80:95], a[108:111], v[100:103], v[80:95]\n"
    "global_load_dwordx4 a[108:111], v61, s[16:17] offset:1024\n"
    "s_waitcnt lgkmcnt(0)\n"
    "v_mfma_f32_32x32x16_bf16 v[64:79], a[112:115], v[104:107], v[64:79]\n"
    "global_load_dwordx4 a[112:115], v61, s[16:17] offset:2048\n"
    "s_waitcnt vmcnt(11)\n"
    "ds_read_b128 v[112:115], v5 offset:4096\n"
    "v_mfma_f32_32x32x16_bf16 v[80:95], a[116:119], v[104:107], v[80:95]\n"
    "ds_read_b128 v[116:119], v5 offset:5120\n"
    "ds_read_b128 v[96:99], v5 offset:6144\n"
    "ds_read_b128 v[100:103], v5 offset:7168\n"
    "v_mfma_f32_32x32x16_bf16 v[64:79], a[120:123], v[108:111], v[64:79]\n"
    "global_load_dwordx4 a[116:119], v61, s[16:17] offset:3072\n"
    "v_mfma_f32_32x32x16_bf16 v[80:95], a[124:127], v[108:111], v[80:95]\n"
    "s_mov_b64 exec, 0xff\n"
    "s_waitcnt vmcnt(11)\n"
    "v_cmp_ne_u32 vcc, s98, v60\n"
    "s_mov_b64 exec, -1\n"
    "s_cbranch_vccz .Lpd11\n"
    "s_mov_b64 exec, 255\n"
    ".Lps11:\n"
    "s_sleep 1\n"
    "buffer_load_dword v60, v11, s[16:19], s82 offen sc1\n"
    "s_waitcnt vmcnt(0)\n"
    "v_cmp_ne_u32 vcc, s98, v60\n"
    "s_nop 1\n"
    "s_cbranch_vccz .Lpe11\n"
    "s_sub_u32 s67, s67, 1\n"
    "s_cbranch_scc0 .Lps11\n"
    "s_mov_b32 s67, 0\n"
    ".Lpe11:\n"
    "s_mov_b64 exec, -1\n"
    ".Lpd11:\n"
    "s_add_u32 m0, s49, 0x10000\n"
    "s_add_u32 s87, s31, 0x1600000\n"
    "v_add_u32 v62, s87, v1\n"
    "global_load_lds_dwordx4 v62, s[16:17] sc1\n"
    "global_load_lds_dwordx4 v62, s[16:17] offset:1024 sc1\n"
    "s_waitcnt lgkmcnt(0)\n"
    "v_mfma_f32_32x32x16_bf16 v[80:95], a[132:135], v[112:115], v[80:95]\n"
    "v_mfma_f32_32x32x16_bf16 v[80:95], a[140:143], v[116:119], v[80:95]\n"
    "v_mfma_f32_32x32x16_bf16 v[80:95], a[148:151], v[96:99], v[80:95]\n"
    "v_mfma_f32_32x32x16_bf16 v[80:95], a[156:159], v[100:103], v[80:95]\n"
    "s_add_u32 m0, s49, 0x12000\n"
    "s_add_u32 s87, s31, 0x1602000\n"
    "v_add_u32 v62, s87, v1\n"
    "global_load_lds_dwordx4 v62, s[16:17] sc1\n"
    "global_load_lds_dwordx4 v62, s[16:17] offset:1024 sc1\n"
    "v_mfma_f32_32x32x16_bf16 v[64:79], a[128:131], v[112:115], v[64:79]\n"
    "s_nop 3\n"
    "ds_write_b128 v5, v[80:83] offset:0\n"
    "v_mfma_f32_32x32x16_bf16 v[64:79], a[136:139], v[116:119], v[64:79]\n"
    "v_mfma_f32_32x32x16_bf16 v[64:79], a[144:147], v[96:99], v[64:79]\n"
    "ds_write_b128 v5, v[84:87] offset:1024\n"
    "v_mfma_f32_32x32x16_bf16 v[64:79], a[152:155], v[100:103], v[64:79]\n"
    "ds_write_b128 v5, v[88:91] offset:2048\n"
    "ds_write_b128 v5, v[92:95] offset:3072\n"
    "s_add_u32 m0, s49, 0x18000\n"
    "s_add_u32 s87, s35, 0x1800000\n"
    "v_add_u32 v62, s87, v1\n"
    "global_load_lds_dwordx4 v62, s[16:17] sc1\n"
    "global_load_lds_dwordx4 v62, s[16:17] offset:1024 sc1\n"
    "s_nop 4\n"
    "ds_write_b128 v5, v[72:75] offset:4096\n"
    "ds_write_b128 v5, v[76:79] offset:5120\n"
    "v_add_f32 v24, v24, v64\n"
    "v_add_f32 v25, v25, v65\n"
    "v_add_f32 v26, v26, v66\n"
    "v_add_f32 v27, v27, v67\n"
    "v_add_f32 v28, v28, v68\n"
    "v_add_f32 v29, v29, v69\n"
    "v_add_f32 v30, v30, v70\n"
    "v_add_f32 v31, v31, v71\n"
    "s_waitcnt lgkmcnt(0)\n"
    "s_barrier\n"
    "s_add_u32 m0, s50, 0x14000\n"
    "s_add_u32 s87, s96, 0x1600000\n"
    "v_add_u32 v62, s87, v1\n"
    "global_load_lds_dwordx4 v62, s[16:17] sc1\n"
    "global_load_lds_dwordx4 v62, s[16:17] offset:1024 sc1\n"
    "global_load_lds_dwordx4 v62, s[16:17] offset:2048 sc1\n"
    "global_load_lds_dwordx4 v62, s[16:17] offset:3072 sc1\n"
    "ds_read_b128 v[104:107], v121 offset:0\n"
    "ds_read_b128 v[108:111], v121 offset:1024\n"
    "ds_read_b128 v[112:115], v122 offset:0\n"
    "ds_read_b128 v[116:119], v122 offset:1024\n"
    "ds_read_b128 v[96:99], v120 offset:0\n"
    "ds_read_b128 v[100:103], v120 offset:1024\n"
    "s_waitcnt lgkmcnt(2)\n"
    "v_add_f32 v104, v104, v112\n"
    "v_add_f32 v105, v105, v113\n"
    "v_add_f32 v106, v106, v114\n"
    "v_add_f32 v107, v107, v115\n"
    "v_add_f32 v108, v108, v116\n"
    "v_add_f32 v109, v109, v117\n"
    "v_add_f32 v110, v110, v118\n"
    "v_add_f32 v111, v111, v119\n"
    "s_waitcnt lgkmcnt(0)\n"
    "v_add_f32 v24, v24, v96\n"
    "v_add_f32 v25, v25, v97\n"
    "v_add_f32 v26, v26, v98\n"
    "v_add_f32 v27, v27, v99\n"
    "v_add_f32 v28, v28, v100\n"
    "v_add_f32 v29, v29, v101\n"
    "v_add_f32 v30, v30, v102\n"
    "v_add_f32 v31, v31, v103\n"
    "v_add_f32 v24, v24, v104\n"
    "v_add_f32 v25, v25, v105\n"
    "v_add_f32 v26, v26, v106\n"
    "v_add_f32 v27, v27, v107\n"
    "v_add_f32 v28, v28, v108\n"
    "v_add_f32 v29, v29, v109\n"
    "v_add_f32 v30, v30, v110\n"
    "v_add_f32 v31, v31, v111\n"
    "v_cvt_pk_bf16_f32 v124, v24, v25\n"
    "v_cvt_pk_bf16_f32 v125, v26, v27\n"
    "v_cvt_pk_bf16_f32 v126, v28, v29\n"
    "v_cvt_pk_bf16_f32 v127, v30, v31\n"
    "s_add_u32 s84, s42, 0x1608000\n"
    "buffer_store_dwordx4 v[124:127], v8, s[16:19], s84 offen sc1\n"
    "v_add_f32 v128, v24, v25\n"
    "v_mul_f32 v129, v24, v24\n"
    "v_add_f32 v130, v26, v27\n"
    "v_mul_f32 v131, v25, v25\n"
    "v_fma_f32 v129, v26, v26, v129\n"
    "v_fma_f32 v131, v27, v27, v131\n"
    "v_add_f32 v128, v128, v28\n"
    "v_add_f32 v130, v130, v29\n"
    "v_fma_f32 v129, v28, v28, v129\n"
    "v_fma_f32 v131, v29, v29, v131\n"
    "v_add_f32 v128, v128, v30\n"
    "v_add_f32 v130, v130, v31\n"
    "v_fma_f32 v129, v30, v30, v129\n"
    "v_fma_f32 v131, v31, v31, v131\n"
    "v_add_f32 v128, v128, v130\n"
    "v_add_f32 v129, v129, v131\n"
    "s_add_u32 s85, s43, 0x1802000\n"
    "v_add_u32 v61, 0x2000, v61\n"
    "global_load_dwordx4 a[120:123], v61, s[16:17] offset:-4096\n"
    "v_permlane32_swap_b32 v128, v129\n"
    "v_add_f32 v128, v128, v129\n"
    "buffer_store_dword v128, v9, s[16:19], s85 offen sc1\n"
    "global_load_dwordx4 a[124:127], v61, s[16:17] offset:-3072\n"
    "global_load_dwordx4 a[132:135], v61, s[16:17] offset:-2048\n"
    "global_load_dwordx4 a[140:143], v61, s[16:17] offset:-1024\n"
    "s_waitcnt vmcnt(14)\n"
    "s_barrier\n"
    "ds_read_b128 v[96:99], v3 offset:16384\n"
    "ds_read_b128 v[100:103], v3 offset:17408\n"
    "ds_read_b128 v[104:107], v3 offset:18432\n"
    "ds_read_b128 v[108:111], v3 offset:19456\n"
    "global_load_dwordx4 a[148:151], v61, s[16:17]\n"
    "global_load_dwordx4 a[156:159], v61, s[16:17] offset:1024\n"
    "s_waitcnt lgkmcnt(2)\n"
    "v_mfma_f32_16x16x32_bf16 v[64:67], a[160:163], v[96:99], 0\n"
    "v_mfma_f32_16x16x32_bf16 v[68:71], a[164:167], v[96:99], 0\n"
    "v_mfma_f32_16x16x32_bf16 v[72:75], a[168:171], v[96:99], 0\n"
    "ds_read_b128 v[96:99], v3 offset:20480\n"
    "global_load_dwordx4 a[128:131], v61, s[16:17] offset:2048\n"
    "v_mfma_f32_16x16x32_bf16 v[64:67], a[172:175], v[100:103], v[64:67]\n"
    "v_mfma_f32_16x16x32_bf16 v[68:71], a[176:179], v[100:103], v[68:71]\n"
    "v_mfma_f32_16x16x32_bf16 v[72:75], a[180:183], v[100:103], v[72:75]\n"
    "ds_read_b128 v[100:103], v3 offset:21504\n"
    "s_waitcnt lgkmcnt(2)\n"
    "v_mfma_f32_16x16x32_bf16 v[64:67], a[184:187], v[104:107], v[64:67]\n"
    "global_load_dwordx4 a[136:139], v61, s[16:17] offset:3072\n"
    "v_mfma_f32_16x16x32_bf16 v[68:71], a[188:191], v[104:107], v[68:71]\n"
    "v_mfma_f32_16x16x32_bf16 v[72:75], a[192:195], v[104:107], v[72:75]\n"
    "ds_read_b128 v[104:107], v3 offset:22528\n"
    "v_mfma_f32_16x16x32_bf16 v[64:67], a[196:199], v[108:111], v[64:67]\n"
    "v_mfma_f32_16x16x32_bf16 v[68:71], a[200:203], v[108:111], v[68:71]\n"
    "v_add_u32 v61, 0x2000, v61\n"
    "global_load_dwordx4 a[144:147], v61, s[16:17] offset:-4096\n"
    "v_mfma_f32_16x16x32_bf16 v[72:75], a[204:207], v[108:111], v[72:75]\n"
    "s_waitcnt vmcnt(15)\n"
    "s_barrier\n"
    "s_waitcnt lgkmcnt(2)\n"
    "v_mfma_f32_16x16x32_bf16 v[64:67], a[208:211], v[96:99], v[64:67]\n"
    "ds_read_b128 v[108:111], v3 offset:23552\n"
    "s_add_u32 s63, s71, 0x18000\n"
    "v_add_u32 v147, s63, v12\n"
    "ds_read_b64 v[128:129], v147 offset:0\n"
    "v_mfma_f32_16x16x32_bf16 v[68:71], a[212:215], v[96:99], v[68:71]\n"
    "ds_read_b64 v[130:131], v147 offset:512\n"
    "ds_read_b64 v[132:133], v147 offset:1024\n"
    "v_mfma_f32_16x16x32_bf16 v[72:75], a[220:223], v[96:99], v[72:75]\n"
    "ds_read_b128 v[96:99], v3 offset:24576\n"
    "ds_read_b64 v[134:135], v147 offset:1536\n"
    "ds_read_b64 v[136:137], v147 offset:2048\n"
    "s_waitcnt lgkmcnt(7)\n"
    "v_mfma_f32_16x16x32_bf16 v[64:67], a[236:239], v[100:103], v[64:67]\n"
    "ds_read_b64 v[138:139], v147 offset:2560\n"
    "ds_read_b64 v[140:141], v147 offset:3072\n"
    "v_mfma_f32_16x16x32_bf16 v[68:71], a[252:255], v[100:103], v[68:71]\n"
    "ds_read_b64 v[142:143], v147 offset:3584\n"
    "global_load_dwordx4 a[152:155], v61, s[16:17] offset:-3072\n"
    "v_mfma_f32_16x16x32_bf16 v[72:75], a[232:235], v[100:103], v[72:75]\n"
    "ds_read_b128 v[100:103], v3 offset:25600\n"
    "v_mfma_f32_16x16x32_bf16 v[64:67], a[248:251], v[104:107], v[64:67]\n"
    "v_mfma_f32_16x16x32_bf16 v[68:71], v[156:159], v[104:107], v[68:71]\n"
    "v_mfma_f32_16x16x32_bf16 v[72:75], a[216:219], v[104:107], v[72:75]\n"
    "ds_read_b128 v[104:107], v3 offset:26624\n"
    "s_waitcnt lgkmcnt(8)\n"
    "v_mfma_f32_16x16x32_bf16 v[64:67], a[228:231], v[108:111], v[64:67]\n"
    "s_waitcnt lgkmcnt(5)\n"
    "v_add_f32 v128, v128, v136\n"
    "v_add_f32 v129, v129, v137\n"
    "s_waitcnt lgkmcnt(3)\n"
    "v_add_f32 v130, v130, v138\n"
    "v_add_f32 v131, v131, v139\n"
    "v_add_f32 v132, v132, v140\n"
    "v_add_f32 v133, v133, v141\n"
    "s_waitcnt lgkmcnt(2)\n"
    "v_add_f32 v134, v134, v142\n"
    "v_add_f32 v135, v135, v143\n"
    "v_add_f32 v128, v128, v132\n"
    "v_add_f32 v129, v129, v133\n"
    "v_add_f32 v130, v130, v134\n"
    "v_add_f32 v131, v131, v135\n"
    "v_mfma_f32_16x16x32_bf16 v[68:71], a[244:247], v[108:111], v[68:71]\n"
    "v_add_f32 v128, v128, v130\n"
    "v_add_f32 v129, v129, v131\n"
    "v_mfma_f32_16x16x32_bf16 v[72:75], a[224:227], v[108:111], v[72:75]\n"
    "ds_read_b128 v[108:111], v3 offset:27648\n"
    "v_permlane32_swap_b32 v128, v129\n"
    "v_mfma_f32_16x16x32_bf16 v[64:67], a[240:243], v[96:99], v[64:67]\n"
    "v_add_f32 v128, v128, v129\n"
    "v_mov_b32 v130, v128\n"
    "v_mfma_f32_16x16x32_bf16 v[68:71], v[152:155], v[96:99], v[68:71]\n"
    "v_permlane16_swap_b32 v128, v130\n"
    "v_mfma_f32_16x16x32_bf16 v[72:75], v[160:163], v[96:99], v[72:75]\n"
    "ds_read_b128 v[96:99], v3 offset:28672\n"
    "v_add_f32 v128, v128, v130\n"
    "v_mov_b32 v129, v128\n"
    "s_waitcnt lgkmcnt(2)\n"
    "v_mfma_f32_16x16x32_bf16 v[64:67], v[164:167], v[100:103], v[64:67]\n"
    "v_permlane32_swap_b32 v128, v129\n"
    "v_mul_f32 v131, s79, v128\n"
    "v_mfma_f32_16x16x32_bf16 v[68:71], v[168:171], v[100:103], v[68:71]\n"
    "v_mov_b32 v132, s81\n"
    "v_fma_f32 v132, v129, s80, v132\n"
    "v_mfma_f32_16x16x32_bf16 v[72:75], v[172:175], v[100:103], v[72:75]\n"
    "ds_read_b128 v[100:103], v3 offset:29696\n"
    "v_fma_f32 v132, -v131, v131, v132\n"
    "v_max_f32 v132, s81, v132\n"
    "v_mfma_f32_16x16x32_bf16 v[64:67], v[176:179], v[104:107], v[64:67]\n"
    "s_waitcnt vmcnt(9)\n"
    "s_barrier\n"
    "v_mov_b32 v62, s98\n"
    "s_add_u32 s83, s47, 0x1f1a080\n"
    "s_mov_b64 exec, s[68:69]\n"
    "buffer_store_dword v62, off, s[16:19], s83 sc1\n"
    "s_mov_b64 exec, -1\n"
    "v_rsq_f32 v133, v132\n"
    "v_mfma_f32_16x16x32_bf16 v[68:71], v[180:183], v[104:107], v[68:71]\n"
    "v_cvt_pk_bf16_f32 v136, v131, v131\n"
    "v_lshlrev_b32 v134, 16, v136\n"
    "v_mfma_f32_16x16x32_bf16 v[72:75], v[184:187], v[104:107], v[72:75]\n"
    "ds_read_b128 v[104:107], v3 offset:30720\n"
    "v_sub_f32 v134, v131, v134\n"
    "v_cvt_pk_bf16_f32 v137, v134, 0\n"
    "s_waitcnt lgkmcnt(2)\n"
    "v_mfma_f32_16x16x32_bf16 v[64:67], v[188:191], v[108:111], v[64:67]\n"
    "v_mul_f32 v135, v132, v133\n"
    "v_cvt_pk_bf16_f32 v138, v135, 0\n"
    "v_lshlrev_b32 v134, 16, v138\n"
    "v_mfma_f32_16x16x32_bf16 v[68:71], v[192:195], v[108:111], v[68:71]\n"
    "v_sub_f32 v134, v135, v134\n"
    "v_cvt_pk_bf16_f32 v139, v134, 0\n"
    "v_mfma_f32_16x16x32_bf16 v[72:75], v[196:199], v[108:111], v[72:75]\n"
    "ds_read_b128 v[108:111], v3 offset:31744\n"
    "v_mul_f32 v133, s78, v133\n"
    "v_add_u32 v147, s73, v150\n"
    "v_mfma_f32_16x16x32_bf16 v[64:67], v[200:203], v[96:99], v[64:67]\n"
    "ds_write_b128 v147, v[136:139] offset:0\n"
    "v_mfma_f32_16x16x32_bf16 v[68:71], v[204:207], v[96:99], v[68:71]\n"
    "v_add_u32 v147, s75, v14\n"
    "v_mfma_f32_16x16x32_bf16 v[72:75], v[208:211], v[96:99], v[72:75]\n"
    "ds_write_b32 v147, v133 offset:1536\n"
    "s_waitcnt lgkmcnt(0)\n"
    "s_barrier\n"
    "v_mfma_f32_16x16x32_bf16 v[64:67], v[212:215], v[100:103], v[64:67]\n"
    "ds_read_b64 v[122:123], v15 offset:1024\n"
    "ds_read_b64 v[120:121], v15 offset:512\n"
    "v_mfma_f32_16x16x32_bf16 v[68:71], v[216:219], v[100:103], v[68:71]\n"
    "ds_read_b128 v[112:115], v3 offset:32768\n"
    "ds_read_b128 v[116:119], v3 offset:33792\n"
    "v_mfma_f32_16x16x32_bf16 v[72:75], v[220:223], v[100:103], v[72:75]\n"
    "ds_read_b32 v144, v14 offset:1600\n"
    "ds_read_b32 v145, v14 offset:1664\n"
    "v_mfma_f32_16x16x32_bf16 v[64:67], v[224:227], v[104:107], v[64:67]\n"
    "ds_read_b128 v[96:99], v3 offset:34816\n"
    "ds_read_b128 v[100:103], v3 offset:35840\n"
    "v_mfma_f32_16x16x32_bf16 v[68:71], v[228:231], v[104:107], v[68:71]\n"
    "v_mfma_f32_16x16x32_bf16 v[72:75], v[232:235], v[104:107], v[72:75]\n"
    "ds_read_b128 v[104:107], v3 offset:36864\n"
    "v_mfma_f32_16x16x32_bf16 v[64:67], v[236:239], v[108:111], v[64:67]\n"
    "v_mfma_f32_16x16x32_bf16 v[68:71], v[240:243], v[108:111], v[68:71]\n"
    "v_mfma_f32_16x16x32_bf16 v[72:75], v[244:247], v[108:111], v[72:75]\n"
    "ds_read_b128 v[108:111], v3 offset:37888\n"
    "s_waitcnt lgkmcnt(7)\n"
    "v_mfma_f32_16x16x16_bf16 v[76:79], v[54:55], v[122:123], 0\n"
    "v_mfma_f32_16x16x16_bf16 v[80:83], v[56:57], v[122:123], 0\n"
    "v_mfma_f32_16x16x16_bf16 v[84:87], v[58:59], v[122:123], 0\n"
    "v_mfma_f32_16x16x16_bf16 v[64:67], v[54:55], v[120:121], v[64:67]\n"
    "v_mfma_f32_16x16x16_bf16 v[68:71], v[56:57], v[120:121], v[68:71]\n"
    "v_mfma_f32_16x16x16_bf16 v[72:75], v[58:59], v[120:121], v[72:75]\n"
    "s_waitcnt lgkmcnt(1)\n"
    "v_mul_f32 v64, v64, v144\n"
    "v_mfma_f32_16x16x32_bf16 v[76:79], a[160:163], v[112:115], v[76:79]\n"
    "v_mul_f32 v65, v65, v144\n"
    "v_mul_f32 v66, v66, v144\n"
    "v_mfma_f32_16x16x32_bf16 v[80:83], a[164:167], v[112:115], v[80:83]\n"
    "v_mul_f32 v67, v67, v144\n"
    "v_exp_f32 v132, v64\n"
    "v_mfma_f32_16x16x32_bf16 v[84:87], a[168:171], v[112:115], v[84:87]\n"
    "ds_read_b128 v[112:115], v3 offset:38912\n"
    "v_exp_f32 v133, v65\n"
    "v_mfma_f32_16x16x32_bf16 v[76:79], a[172:175], v[116:119], v[76:79]\n"
    "v_exp_f32 v134, v66\n"
    "v_mfma_f32_16x16x32_bf16 v[80:83], a[176:179], v[116:119], v[80:83]\n"
    "v_exp_f32 v135, v67\n"
    "v_mfma_f32_16x16x32_bf16 v[84:87], a[180:183], v[116:119], v[84:87]\n"
    "ds_read_b128 v[116:119], v3 offset:39936\n"
    "v_fma_f32 v136, v132, 0.5, 1.0\n"
    "v_mfma_f32_16x16x32_bf16 v[76:79], a[184:187], v[96:99], v[76:79]\n"
    "v_fma_f32 v137, v133, 0.5, 1.0\n"
    "v_fma_f32 v138, v134, 0.5, 1.0\n"
    "v_mfma_f32_16x16x32_bf16 v[80:83], a[188:191], v[96:99], v[80:83]\n"
    "v_fma_f32 v139, v135, 0.5, 1.0\n"
    "v_fma_f32 v136, v132, v136, 1.0\n"
    "v_mfma_f32_16x16x32_bf16 v[84:87], a[192:195], v[96:99], v[84:87]\n"
    "ds_read_b128 v[96:99], v3 offset:40960\n"
    "v_fma_f32 v137, v133, v137, 1.0\n"
    "v_fma_f32 v138, v134, v138, 1.0\n"
    "v_fma_f32 v139, v135, v139, 1.0\n"
    "v_mfma_f32_16x16x32_bf16 v[76:79], a[196:199], v[100:103], v[76:79]\n"
    "v_rcp_f32 v136, v136\n"
    "v_mfma_f32_16x16x32_bf16 v[80:83], a[200:203], v[100:103], v[80:83]\n"
    "v_rcp_f32 v137, v137\n"
    "v_mfma_f32_16x16x32_bf16 v[84:87], a[204:207], v[100:103], v[84:87]\n"
    "ds_read_b128 v[100:103], v3 offset:41984\n"
    "v_rcp_f32 v138, v138\n"
    "v_mfma_f32_16x16x32_bf16 v[76:79], a[208:211], v[104:107], v[76:79]\n"
    "v_rcp_f32 v139, v139\n"
    "s_mov_b32 s82, 0x1f1a080\n"
    "s_mov_b64 exec, 0xff\n"
    "buffer_load_dword v60, v11, s[16:19], s82 offen sc1\n"
    "s_mov_b64 exec, -1\n"
    "v_mfma_f32_16x16x32_bf16 v[80:83], a[212:215], v[104:107], v[80:83]\n"
    "v_fma_f32 v64, -v64, v136, v64\n"
    "v_fma_f32 v65, -v65, v137, v65\n"
    "v_mfma_f32_16x16x32_bf16 v[84:87], a[220:223], v[104:107], v[84:87]\n"
    "ds_read_b128 v[104:107], v3 offset:43008\n"
    "v_fma_f32 v66, -v66, v138, v66\n"
    "v_fma_f32 v67, -v67, v139, v67\n"
    "s_waitcnt lgkmcnt(2)\n"
    "v_mfma_f32_16x16x32_bf16 v[76:79], a[236:239], v[108:111], v[76:79]\n"
    "v_cvt_pk_bf16_f32 v120, v64, v65\n"
    "v_cvt_pk_bf16_f32 v121, v66, v67\n"
    "v_mfma_f32_16x16x32_bf16 v[80:83], a[252:255], v[108:111], v[80:83]\n"
    "v_mul_f32 v68, v68, v144\n"
    "v_mul_f32 v69, v69, v144\n"
    "v_mul_f32 v70, v70, v144\n"
    "v_mfma_f32_16x16x32_bf16 v[84:87], a[232:235], v[108:111], v[84:87]\n"
    "ds_read_b128 v[108:111], v3 offset:44032\n"
    "v_mul_f32 v71, v71, v144\n"
    "v_exp_f32 v132, v68\n"
    "v_mfma_f32_16x16x32_bf16 v[76:79], a[248:251], v[112:115], v[76:79]\n"
    "v_exp_f32 v133, v69\n"
    "v_mfma_f32_16x16x32_bf16 v[80:83], v[156:159], v[112:115], v[80:83]\n"
    "v_exp_f32 v134, v70\n"
    "v_mfma_f32_16x16x32_bf16 v[84:87], a[216:219], v[112:115], v[84:87]\n"
    "ds_read_b128 v[112:115], v3 offset:45056\n"
    "v_exp_f32 v135, v71\n"
    "v_mfma_f32_16x16x32_bf16 v[76:79], a[228:231], v[116:119], v[76:79]\n"
    "v_fma_f32 v136, v132, 0.5, 1.0\n"
    "v_fma_f32 v137, v133, 0.5, 1.0\n"
    "v_mfma_f32_16x16x32_bf16 v[80:83], a[244:247], v[116:119], v[80:83]\n"
    "v_fma_f32 v138, v134, 0.5, 1.0\n"
    "v_fma_f32 v139, v135, 0.5, 1.0\n"
    "v_mfma_f32_16x16x32_bf16 v[84:87], a[224:227], v[116:119], v[84:87]\n"
    "ds_read_b128 v[116:119], v3 offset:46080\n"
    "v_fma_f32 v136, v132, v136, 1.0\n"
    "v_mfma_f32_16x16x32_bf16 v[76:79], a[240:243], v[96:99], v[76:79]\n"
    "v_fma_f32 v137, v133, v137, 1.0\n"
    "v_fma_f32 v138, v134, v138, 1.0\n"
    "v_fma_f32 v139, v135, v139, 1.0\n"
    "v_mfma_f32_16x16x32_bf16 v[80:83], v[152:155], v[96:99], v[80:83]\n"
    "v_rcp_f32 v136, v136\n"
    "v_mfma_f32_16x16x32_bf16 v[84:87], v[160:163], v[96:99], v[84:87]\n"
    "ds_read_b128 v[96:99], v3 offset:47104\n"
    "v_rcp_f32 v137, v137\n"
    "s_waitcnt lgkmcnt(2)\n"
    "v_mfma_f32_16x16x32_bf16 v[76:79], v[164:167], v[100:103], v[76:79]\n"
    "v_rcp_f32 v138, v138\n"
    "v_mfma_f32_16x16x32_bf16 v[80:83], v[168:171], v[100:103], v[80:83]\n"
    "v_rcp_f32 v139, v139\n"
    "v_mfma_f32_16x16x32_bf16 v[84:87], v[172:175], v[100:103], v[84:87]\n"
    "ds_read_b128 v[100:103], v3 offset:48128\n"
    "v_fma_f32 v68, -v68, v136, v68\n"
    "v_fma_f32 v69, -v69, v137, v69\n"
    "v_mfma_f32_16x16x32_bf16 v[76:79], v[176:179], v[104:107], v[76:79]\n"
    "v_fma_f32 v70, -v70, v138, v70\n"
    "v_fma_f32 v71, -v71, v139, v71\n"
    "v_mfma_f32_16x16x32_bf16 v[80:83], v[180:183], v[104:107], v[80:83]\n"
    "v_cvt_pk_bf16_f32 v124, v68, v69\n"
    "v_cvt_pk_bf16_f32 v125, v70, v71\n"
    "v_mfma_f32_16x16x32_bf16 v[84:87], v[184:187], v[104:107], v[84:87]\n"
    "v_mul_f32 v72, v72, v144\n"
    "v_mul_f32 v73, v73, v144\n"
    "v_mul_f32 v74, v74, v144\n"
    "v_mfma_f32_16x16x32_bf16 v[76:79], v[188:191], v[108:111], v[76:79]\n"
    "v_mul_f32 v75, v75, v144\n"
    "v_exp_f32 v132, v72\n"
    "v_mfma_f32_16x16x32_bf16 v[80:83], v[192:195], v[108:111], v[80:83]\n"
    "v_exp_f32 v133, v73\n"
    "v_mfma_f32_16x16x32_bf16 v[84:87], v[196:199], v[108:111], v[84:87]\n"
    "v_exp_f32 v134, v74\n"
    "v_mfma_f32_16x16x32_bf16 v[76:79], v[200:203], v[112:115], v[76:79]\n"
    "v_exp_f32 v135, v75\n"
    "v_mfma_f32_16x16x32_bf16 v[80:83], v[204:207], v[112:115], v[80:83]\n"
    "v_fma_f32 v136, v132, 0.5, 1.0\n"
    "v_fma_f32 v137, v133, 0.5, 1.0\n"
    "v_mfma_f32_16x16x32_bf16 v[84:87], v[208:211], v[112:115], v[84:87]\n"
    "v_fma_f32 v138, v134, 0.5, 1.0\n"
    "s_waitcnt lgkmcnt(0)\n"
    "v_mfma_f32_16x16x32_bf16 v[76:79], v[212:215], v[116:119], v[76:79]\n"
    "v_fma_f32 v139, v135, 0.5, 1.0\n"
    "v_fma_f32 v136, v132, v136, 1.0\n"
    "v_mfma_f32_16x16x32_bf16 v[80:83], v[216:219], v[116:119], v[80:83]\n"
    "v_fma_f32 v137, v133, v137, 1.0\n"
    "v_fma_f32 v138, v134, v138, 1.0\n"
    "v_fma_f32 v139, v135, v139, 1.0\n"
    "v_mfma_f32_16x16x32_bf16 v[84:87], v[220:223], v[116:119], v[84:87]\n"
    "v_rcp_f32 v136, v136\n"
    "v_mfma_f32_16x16x32_bf16 v[76:79], v[224:227], v[96:99], v[76:79]\n"
    "v_rcp_f32 v137, v137\n"
    "v_mfma_f32_16x16x32_bf16 v[80:83], v[228:231], v[96:99], v[80:83]\n"
    "v_rcp_f32 v138, v138\n"
    "v_mfma_f32_16x16x32_bf16 v[84:87], v[232:235], v[96:99], v[84:87]\n"
    "v_rcp_f32 v139, v139\n"
    "v_mfma_f32_16x16x32_bf16 v[76:79], v[236:239], v[100:103], v[76:79]\n"
    "v_fma_f32 v72, -v72, v136, v72\n"
    "v_fma_f32 v73, -v73, v137, v73\n"
    "v_mfma_f32_16x16x32_bf16 v[80:83], v[240:243], v[100:103], v[80:83]\n"
    "v_fma_f32 v74, -v74, v138, v74\n"
    "v_fma_f32 v75, -v75, v139, v75\n"
    "v_mfma_f32_16x16x32_bf16 v[84:87], v[244:247], v[100:103], v[84:87]\n"
    "v_cvt_pk_bf16_f32 v128, v72, v73\n"
    "v_cvt_pk_bf16_f32 v129, v74, v75\n"
    "v_mul_f32 v76, v76, v145\n"
    "v_mul_f32 v77, v77, v145\n"
    "v_mul_f32 v78, v78, v145\n"
    "v_mul_f32 v79, v79, v145\n"
    "v_exp_f32 v132, v76\n"
    "v_exp_f32 v133, v77\n"
    "v_exp_f32 v134, v78\n"
    "v_exp_f32 v135, v79\n"
    "v_fma_f32 v136, v132, 0.5, 1.0\n"
    "v_fma_f32 v137, v133, 0.5, 1.0\n"
    "v_fma_f32 v138, v134, 0.5, 1.0\n"
    "v_fma_f32 v139, v135, 0.5, 1.0\n"
    "v_fma_f32 v136, v132, v136, 1.0\n"
    "v_fma_f32 v137, v133, v137, 1.0\n"
    "v_fma_f32 v138, v134, v138, 1.0\n"
    "v_fma_f32 v139, v135, v139, 1.0\n"
    "v_rcp_f32 v136, v136\n"
    "v_rcp_f32 v137, v137\n"
    "v_rcp_f32 v138, v138\n"
    "v_rcp_f32 v139, v139\n"
    "v_fma_f32 v76, -v76, v136, v76\n"
    "v_fma_f32 v77, -v77, v137, v77\n"
    "v_fma_f32 v78, -v78, v138, v78\n"
    "v_fma_f32 v79, -v79, v139, v79\n"
    "v_cvt_pk_bf16_f32 v122, v76, v77\n"
    "v_cvt_pk_bf16_f32 v123, v78, v79\n"
    "s_nop 0\n"
    "v_permlane16_swap_b32 v120, v122\n"
    "v_permlane16_swap_b32 v121, v123\n"
    "s_add_u32 s84, s45, 0x1880000\n"
    "buffer_store_dwordx4 v[120:123], v1, s[16:19], s84 offen sc1\n"
    "v_mul_f32 v80, v80, v145\n"
    "v_mul_f32 v81, v81, v145\n"
    "v_mul_f32 v82, v82, v145\n"
    "v_mul_f32 v83, v83, v145\n"
    "v_exp_f32 v132, v80\n"
    "v_exp_f32 v133, v81\n"
    "v_exp_f32 v134, v82\n"
    "v_exp_f32 v135, v83\n"
    "v_fma_f32 v136, v132, 0.5, 1.0\n"
    "v_fma_f32 v137, v133, 0.5, 1.0\n"
    "v_fma_f32 v138, v134, 0.5, 1.0\n"
    "v_fma_f32 v139, v135, 0.5, 1.0\n"
    "v_fma_f32 v136, v132, v136, 1.0\n"
    "v_fma_f32 v137, v133, v137, 1.0\n"
    "v_fma_f32 v138, v134, v138, 1.0\n"
    "v_fma_f32 v139, v135, v139, 1.0\n"
    "v_rcp_f32 v136, v136\n"
    "s_mov_b64 exec, 0xff\n"
    "s_waitcnt vmcnt(1)\n"
    "v_cmp_ne_u32 vcc, s98, v60\n"
    "s_mov_b64 exec, -1\n"
    "s_cbranch_vccz .Lpd12\n"
    "s_mov_b64 exec, 255\n"
    ".Lps12:\n"
    "s_sleep 1\n"
    "buffer_load_dword v60, v11, s[16:19], s82 offen sc1\n"
    "s_waitcnt vmcnt(0)\n"
    "v_cmp_ne_u32 vcc, s98, v60\n"
    "s_nop 1\n"
    "s_cbranch_vccz .Lpe12\n"
    "s_sub_u32 s67, s67, 1\n"
    "s_cbranch_scc0 .Lps12\n"
    "s_mov_b32 s67, 0\n"
    ".Lpe12:\n"
    "s_mov_b64 exec, -1\n"
    ".Lpd12:\n"
    "s_add_u32 m0, s49, 0x1d000\n"
    "s_add_u32 s87, s31, 0x1608000\n"
    "v_add_u32 v62, s87, v1\n"
    "global_load_lds_dwordx4 v62, s[16:17] sc1\n"
    "global_load_lds_dwordx4 v62, s[16:17] offset:1024 sc1\n"
    "s_add_u32 m0, s49, 0x1f000\n"
    "s_add_u32 s87, s31, 0x160a000\n"
    "v_add_u32 v62, s87, v1\n"
    "global_load_lds_dwordx4 v62, s[16:17] sc1\n"
    "global_load_lds_dwordx4 v62, s[16:17] offset:1024 sc1\n"
    "v_rcp_f32 v137, v137\n"
    "v_rcp_f32 v138, v138\n"
    "v_rcp_f32 v139, v139\n"
    "v_fma_f32 v80, -v80, v136, v80\n"
    "v_fma_f32 v81, -v81, v137, v81\n"
    "v_fma_f32 v82, -v82, v138, v82\n"
    "v_fma_f32 v83, -v83, v139, v83\n"
    "v_cvt_pk_bf16_f32 v126, v80, v81\n"
    "v_cvt_pk_bf16_f32 v127, v82, v83\n"
    "s_nop 0\n"
    "v_permlane16_swap_b32 v124, v126\n"
    "v_permlane16_swap_b32 v125, v127\n"
    "buffer_store_dwordx4 v[124:127], v1, s[16:19], s84 offen offset:1024 sc1\n"
    "v_mul_f32 v84, v84, v145\n"
    "v_mul_f32 v85, v85, v145\n"
    "v_mul_f32 v86, v86, v145\n"
    "v_mul_f32 v87, v87, v145\n"
    "v_exp_f32 v132, v84\n"
    "v_exp_f32 v133, v85\n"
    "v_exp_f32 v134, v86\n"
    "v_exp_f32 v135, v87\n"
    "v_fma_f32 v136, v132, 0.5, 1.0\n"
    "v_fma_f32 v137, v133, 0.5, 1.0\n"
    "v_fma_f32 v138, v134, 0.5, 1.0\n"
    "v_fma_f32 v139, v135, 0.5, 1.0\n"
    "v_fma_f32 v136, v132, v136, 1.0\n"
    "v_fma_f32 v137, v133, v137, 1.0\n"
    "v_fma_f32 v138, v134, v138, 1.0\n"
    "v_fma_f32 v139, v135, v139, 1.0\n"
    "v_rcp_f32 v136, v136\n"
    "s_add_u32 m0, s49, 0x25000\n"
    "s_add_u32 s87, s35, 0x1802000\n"
    "v_add_u32 v62, s87, v1\n"
    "global_load_lds_dwordx4 v62, s[16:17] sc1\n"
    "global_load_lds_dwordx4 v62, s[16:17] offset:1024 sc1\n"
    "v_rcp_f32 v137, v137\n"
    "v_rcp_f32 v138, v138\n"
    "v_rcp_f32 v139, v139\n"
    "v_fma_f32 v84, -v84, v136, v84\n"
    "v_fma_f32 v85, -v85, v137, v85\n"
    "v_fma_f32 v86, -v86, v138, v86\n"
    "v_fma_f32 v87, -v87, v139, v87\n"
    "v_cvt_pk_bf16_f32 v130, v84, v85\n"
    "v_cvt_pk_bf16_f32 v131, v86, v87\n"
    "s_nop 0\n"
    "v_permlane16_swap_b32 v128, v130\n"
    "v_permlane16_swap_b32 v129, v131\n"
    "buffer_store_dwordx4 v[128:131], v151, s[16:19], s84 offen offset:2048 sc1\n"
    "s_add_u32 m0, s50, 0x21000\n"
    "s_add_u32 s87, s96, 0x1608000\n"
    "v_add_u32 v62, s87, v1\n"
    "global_load_lds_dwordx4 v62, s[16:17] sc1\n"
    "global_load_lds_dwordx4 v62, s[16:17] offset:1024 sc1\n"
    "global_load_lds_dwordx4 v62, s[16:17] offset:2048 sc1\n"
    "global_load_lds_dwordx4 v62, s[16:17] offset:3072 sc1\n"
    "s_waitcnt vmcnt(10)\n"
    "s_barrier\n"
    "ds_read_b128 v[96:99], v4 offset:20480\n"
    "ds_read_b128 v[100:103], v4 offset:21504\n"
    "ds_read_b128 v[104:107], v4 offset:22528\n"
    "ds_read_b128 v[108:111], v4 offset:23552\n"
    "s_waitcnt lgkmcnt(2)\n"
    "v_mfma_f32_16x16x32_bf16 v[64:67], a[160:163], v[96:99], 0\n"
    "v_mfma_f32_16x16x32_bf16 v[68:71], a[164:167], v[96:99], 0\n"
    "v_mfma_f32_16x16x32_bf16 v[72:75], a[168:171], v[96:99], 0\n"
    "ds_read_b128 v[96:99], v4 offset:24576\n"
    "v_mfma_f32_16x16x32_bf16 v[64:67], a[172:175], v[100:103], v[64:67]\n"
    "v_mfma_f32_16x16x32_bf16 v[68:71], a[176:179], v[100:103], v[68:71]\n"
    "v_mfma_f32_16x16x32_bf16 v[72:75], a[180:183], v[100:103], v[72:75]\n"
    "ds_read_b128 v[100:103], v4 offset:25600\n"
    "s_waitcnt lgkmcnt(2)\n"
    "v_mfma_f32_16x16x32_bf16 v[64:67], a[184:187], v[104:107], v[64:67]\n"
    "v_mfma_f32_16x16x32_bf16 v[68:71], a[188:191], v[104:107], v[68:71]\n"
    "v_mfma_f32_16x16x32_bf16 v[72:75], a[192:195], v[104:107], v[72:75]\n"
    "ds_read_b128 v[104:107], v4 offset:26624\n"
    "v_mfma_f32_16x16x32_bf16 v[64:67], a[196:199], v[108:111], v[64:67]\n"
    "v_mfma_f32_16x16x32_bf16 v[68:71], a[200:203], v[108:111], v[68:71]\n"
    "v_mfma_f32_16x16x32_bf16 v[72:75], a[204:207], v[108:111], v[72:75]\n"
    "ds_read_b128 v[108:111], v4 offset:27648\n"
    "s_waitcnt vmcnt(4)\n"
    "s_barrier\n"
    "s_barrier\n"
    "v_mov_b32 v62, s98\n"
    "s_add_u32 s83, s47, 0x1f1c000\n"
    "s_mov_b64 exec, s[68:69]\n"
    "buffer_store_dword v62, off, s[16:19], s83 sc1\n"
    "s_mov_b64 exec, -1\n"
    "s_waitcnt lgkmcnt(1)\n"
    "v_mfma_f32_16x16x32_bf16 v[64:67], a[208:211], v[96:99], v[64:67]\n"
    "s_add_u32 s63, s71, 0x25000\n"
    "v_add_u32 v147, s63, v12\n"
    "ds_read_b64 v[128:129], v147 offset:0\n"
    "v_mfma_f32_16x16x32_bf16 v[68:71], a[212:215], v[96:99], v[68:71]\n"
    "ds_read_b64 v[130:131], v147 offset:512\n"
    "ds_read_b64 v[132:133], v147 offset:1024\n"
    "v_mfma_f32_16x16x32_bf16 v[72:75], a[220:223], v[96:99], v[72:75]\n"
    "ds_read_b128 v[96:99], v4 offset:28672\n"
    "ds_read_b64 v[134:135], v147 offset:1536\n"
    "ds_read_b64 v[136:137], v147 offset:2048\n"
    "v_mfma_f32_16x16x32_bf16 v[64:67], a[236:239], v[100:103], v[64:67]\n"
    "ds_read_b64 v[138:139], v147 offset:2560\n"
    "ds_read_b64 v[140:141], v147 offset:3072\n"
    "v_mfma_f32_16x16x32_bf16 v[68:71], a[252:255], v[100:103], v[68:71]\n"
    "ds_read_b64 v[142:143], v147 offset:3584\n"
    "v_mfma_f32_16x16x32_bf16 v[72:75], a[232:235], v[100:103], v[72:75]\n"
    "ds_read_b128 v[100:103], v4 offset:29696\n"
    "v_mfma_f32_16x16x32_bf16 v[64:67], a[248:251], v[104:107], v[64:67]\n"
    "v_mfma_f32_16x16x32_bf16 v[68:71], v[156:159], v[104:107], v[68:71]\n"
    "v_mfma_f32_16x16x32_bf16 v[72:75], a[216:219], v[104:107], v[72:75]\n"
    "ds_read_b128 v[104:107], v4 offset:30720\n"
    "s_waitcnt lgkmcnt(8)\n"
    "v_mfma_f32_16x16x32_bf16 v[64:67], a[228:231], v[108:111], v[64:67]\n"
    "v_mfma_f32_16x16x32_bf16 v[68:71], a[244:247], v[108:111], v[68:71]\n"
    "s_waitcnt lgkmcnt(4)\n"
    "v_add_f32 v128, v128, v136\n"
    "v_add_f32 v129, v129, v137\n"
    "v_add_f32 v130, v130, v138\n"
    "v_add_f32 v131, v131, v139\n"
    "s_waitcnt lgkmcnt(2)\n"
    "v_add_f32 v132, v132, v140\n"
    "v_add_f32 v133, v133, v141\n"
    "v_add_f32 v134, v134, v142\n"
    "v_add_f32 v135, v135, v143\n"
    "v_add_f32 v128, v128, v132\n"
    "v_add_f32 v129, v129, v133\n"
    "v_add_f32 v130, v130, v134\n"
    "v_add_f32 v131, v131, v135\n"
    "v_add_f32 v128, v128, v130\n"
    "v_add_f32 v129, v129, v131\n"
    "v_mfma_f32_16x16x32_bf16 v[72:75], a[224:227], v[108:111], v[72:75]\n"
    "ds_read_b128 v[108:111], v4 offset:31744\n"
    "v_permlane32_swap_b32 v128, v129\n"
    "v_mfma_f32_16x16x32_bf16 v[64:67], a[240:243], v[96:99], v[64:67]\n"
    "v_add_f32 v128, v128, v129\n"
    "v_mov_b32 v130, v128\n"
    "v_mfma_f32_16x16x32_bf16 v[68:71], v[152:155], v[96:99], v[68:71]\n"
    "v_permlane16_swap_b32 v128, v130\n"
    "v_mfma_f32_16x16x32_bf16 v[72:75], v[160:163], v[96:99], v[72:75]\n"
    "ds_read_b128 v[96:99], v4 offset:32768\n"
    "v_add_f32 v128, v128, v130\n"
    "v_mov_b32 v129, v128\n"
    "s_waitcnt lgkmcnt(2)\n"
    "v_mfma_f32_16x16x32_bf16 v[64:67], v[164:167], v[100:103], v[64:67]\n"
    "v_permlane32_swap_b32 v128, v129\n"
    "v_mul_f32 v131, s79, v128\n"
    "v_mfma_f32_16x16x32_bf16 v[68:71], v[168:171], v[100:103], v[68:71]\n"
    "v_mov_b32 v132, s81\n"
    "v_fma_f32 v132, v129, s80, v132\n"
    "v_mfma_f32_16x16x32_bf16 v[72:75], v[172:175], v[100:103], v[72:75]\n"
    "ds_read_b128 v[100:103], v4 offset:33792\n"
    "v_fma_f32 v132, -v131, v131, v132\n"
    "v_max_f32 v132, s81, v132\n"
    "v_mfma_f32_16x16x32_bf16 v[64:67], v[176:179], v[104:107], v[64:67]\n"
    "v_rsq_f32 v133, v132\n"
    "v_mfma_f32_16x16x32_bf16 v[68:71], v[180:183], v[104:107], v[68:71]\n"
    "v_cvt_pk_bf16_f32 v136, v131, v131\n"
    "v_lshlrev_b32 v134, 16, v136\n"
    "v_mfma_f32_16x16x32_bf16 v[72:75], v[184:187], v[104:107], v[72:75]\n"
    "ds_read_b128 v[104:107], v4 offset:34816\n"
    "v_sub_f32 v134, v131, v134\n"
    "v_cvt_pk_bf16_f32 v137, v134, 0\n"
    "s_waitcnt lgkmcnt(2)\n"
    "v_mfma_f32_16x16x32_bf16 v[64:67], v[188:191], v[108:111], v[64:67]\n"
    "v_mul_f32 v135, v132, v133\n"
    "v_cvt_pk_bf16_f32 v138, v135, 0\n"
    "v_lshlrev_b32 v134, 16, v138\n"
    "v_mfma_f32_16x16x32_bf16 v[68:71], v[192:195], v[108:111], v[68:71]\n"
    "v_sub_f32 v134, v135, v134\n"
    "v_cvt_pk_bf16_f32 v139, v134, 0\n"
    "v_mfma_f32_16x16x32_bf16 v[72:75], v[196:199], v[108:111], v[72:75]\n"
    "ds_read_b128 v[108:111], v4 offset:35840\n"
    "v_mul_f32 v133, s78, v133\n"
    "v_add_u32 v147, s73, v150\n"
    "v_mfma_f32_16x16x32_bf16 v[64:67], v[200:203], v[96:99], v[64:67]\n"
    "ds_write_b128 v147, v[136:139] offset:2048\n"
    "v_mfma_f32_16x16x32_bf16 v[68:71], v[204:207], v[96:99], v[68:71]\n"
    "v_add_u32 v147, s75, v14\n"
    "v_mfma_f32_16x16x32_bf16 v[72:75], v[208:211], v[96:99], v[72:75]\n"
    "ds_write_b32 v147, v133 offset:3584\n"
    "s_waitcnt vmcnt(1)\n"
    "s_waitcnt lgkmcnt(0)\n"
    "s_barrier\n"
    "v_mfma_f32_16x16x32_bf16 v[64:67], v[212:215], v[100:103], v[64:67]\n"
    "ds_read_b64 v[122:123], v15 offset:3072\n"
    "ds_read_b64 v[120:121], v15 offset:2560\n"
    "v_mfma_f32_16x16x32_bf16 v[68:71], v[216:219], v[100:103], v[68:71]\n"
    "ds_read_b128 v[112:115], v4 offset:36864\n"
    "ds_read_b128 v[116:119], v4 offset:37888\n"
    "v_mfma_f32_16x16x32_bf16 v[72:75], v[220:223], v[100:103], v[72:75]\n"
    "ds_read_b32 v144, v14 offset:3648\n"
    "ds_read_b32 v145, v14 offset:3712\n"
    "v_mfma_f32_16x16x32_bf16 v[64:67], v[224:227], v[104:107], v[64:67]\n"
    "ds_read_b128 v[96:99], v4 offset:38912\n"
    "ds_read_b128 v[100:103], v4 offset:39936\n"
    "v_mfma_f32_16x16x32_bf16 v[68:71], v[228:231], v[104:107], v[68:71]\n"
    "v_mfma_f32_16x16x32_bf16 v[72:75], v[232:235], v[104:107], v[72:75]\n"
    "ds_read_b128 v[104:107], v4 offset:40960\n"
    "s_mov_b32 s82, 0x1f1c000\n"
    "s_mov_b64 exec, 0xff\n"
    "buffer_load_dword v60, v11, s[16:19], s82 offen sc1\n"
    "s_mov_b64 exec, -1\n"
    "v_mfma_f32_16x16x32_bf16 v[64:67], v[236:239], v[108:111], v[64:67]\n"
    "v_mfma_f32_16x16x32_bf16 v[68:71], v[240:243], v[108:111], v[68:71]\n"
    "v_mfma_f32_16x16x32_bf16 v[72:75], v[244:247], v[108:111], v[72:75]\n"
    "ds_read_b128 v[108:111], v4 offset:41984\n"
    "s_waitcnt lgkmcnt(4)\n"
    "v_mfma_f32_16x16x16_bf16 v[76:79], v[54:55], v[122:123], 0\n"
    "v_mfma_f32_16x16x16_bf16 v[80:83], v[56:57], v[122:123], 0\n"
    "v_mfma_f32_16x16x16_bf16 v[84:87], v[58:59], v[122:123], 0\n"
    "v_mfma_f32_16x16x16_bf16 v[64:67], v[54:55], v[120:121], v[64:67]\n"
    "v_mfma_f32_16x16x16_bf16 v[68:71], v[56:57], v[120:121], v[68:71]\n"
    "v_mfma_f32_16x16x16_bf16 v[72:75], v[58:59], v[120:121], v[72:75]\n"
    "v_mul_f32 v64, v64, v144\n"
    "v_mul_f32 v65, v65, v144\n"
    "v_mfma_f32_16x16x32_bf16 v[76:79], a[160:163], v[112:115], v[76:79]\n"
    "v_mul_f32 v66, v66, v144\n"
    "v_mul_f32 v67, v67, v144\n"
    "v_mfma_f32_16x16x32_bf16 v[80:83], a[164:167], v[112:115], v[80:83]\n"
    "v_exp_f32 v132, v64\n"
    "v_mfma_f32_16x16x32_bf16 v[84:87], a[168:171], v[112:115], v[84:87]\n"
    "ds_read_b128 v[112:115], v4 offset:43008\n"
    "v_exp_f32 v133, v65\n"
    "v_mfma_f32_16x16x32_bf16 v[76:79], a[172:175], v[116:119], v[76:79]\n"
    "v_exp_f32 v134, v66\n"
    "v_mfma_f32_16x16x32_bf16 v[80:83], a[176:179], v[116:119], v[80:83]\n"
    "v_exp_f32 v135, v67\n"
    "v_mfma_f32_16x16x32_bf16 v[84:87], a[180:183], v[116:119], v[84:87]\n"
    "ds_read_b128 v[116:119], v4 offset:44032\n"
    "v_fma_f32 v136, v132, 0.5, 1.0\n"
    "s_waitcnt lgkmcnt(2)\n"
    "v_mfma_f32_16x16x32_bf16 v[76:79], a[184:187], v[96:99], v[76:79]\n"
    "v_fma_f32 v137, v133, 0.5, 1.0\n"
    "v_fma_f32 v138, v134, 0.5, 1.0\n"
    "v_mfma_f32_16x16x32_bf16 v[80:83], a[188:191], v[96:99], v[80:83]\n"
    "v_fma_f32 v139, v135, 0.5, 1.0\n"
    "v_fma_f32 v136, v132, v136, 1.0\n"
    "v_mfma_f32_16x16x32_bf16 v[84:87], a[192:195], v[96:99], v[84:87]\n"
    "ds_read_b128 v[96:99], v4 offset:45056\n"
    "v_fma_f32 v137, v133, v137, 1.0\n"
    "v_fma_f32 v138, v134, v138, 1.0\n"
    "v_fma_f32 v139, v135, v139, 1.0\n"
    "v_mfma_f32_16x16x32_bf16 v[76:79], a[196:199], v[100:103], v[76:79]\n"
    "v_rcp_f32 v136, v136\n"
    "v_mfma_f32_16x16x32_bf16 v[80:83], a[200:203], v[100:103], v[80:83]\n"
    "v_rcp_f32 v137, v137\n"
    "v_mfma_f32_16x16x32_bf16 v[84:87], a[204:207], v[100:103], v[84:87]\n"
    "ds_read_b128 v[100:103], v4 offset:46080\n"
    "v_rcp_f32 v138, v138\n"
    "v_mfma_f32_16x16x32_bf16 v[76:79], a[208:211], v[104:107], v[76:79]\n"
    "v_rcp_f32 v139, v139\n"
    "v_mfma_f32_16x16x32_bf16 v[80:83], a[212:215], v[104:107], v[80:83]\n"
    "v_fma_f32 v64, -v64, v136, v64\n"
    "v_fma_f32 v65, -v65, v137, v65\n"
    "v_mfma_f32_16x16x32_bf16 v[84:87], a[220:223], v[104:107], v[84:87]\n"
    "ds_read_b128 v[104:107], v4 offset:47104\n"
    "v_fma_f32 v66, -v66, v138, v66\n"
    "v_fma_f32 v67, -v67, v139, v67\n"
    "v_mfma_f32_16x16x32_bf16 v[76:79], a[236:239], v[108:111], v[76:79]\n"
    "v_cvt_pk_bf16_f32 v120, v64, v65\n"
    "v_cvt_pk_bf16_f32 v121, v66, v67\n"
    "v_mfma_f32_16x16x32_bf16 v[80:83], a[252:255], v[108:111], v[80:83]\n"
    "v_mul_f32 v68, v68, v144\n"
    "v_mul_f32 v69, v69, v144\n"
    "v_mul_f32 v70, v70, v144\n"
    "v_mfma_f32_16x16x32_bf16 v[84:87], a[232:235], v[108:111], v[84:87]\n"
    "ds_read_b128 v[108:111], v4 offset:48128\n"
    "v_mul_f32 v71, v71, v144\n"
    "v_exp_f32 v132, v68\n"
    "s_waitcnt lgkmcnt(2)\n"
    "v_mfma_f32_16x16x32_bf16 v[76:79], a[248:251], v[112:115], v[76:79]\n"
    "v_exp_f32 v133, v69\n"
    "v_mfma_f32_16x16x32_bf16 v[80:83], v[156:159], v[112:115], v[80:83]\n"
    "v_exp_f32 v134, v70\n"
    "v_mfma_f32_16x16x32_bf16 v[84:87], a[216:219], v[112:115], v[84:87]\n"
    "ds_read_b128 v[112:115], v4 offset:49152\n"
    "v_exp_f32 v135, v71\n"
    "v_mfma_f32_16x16x32_bf16 v[76:79], a[228:231], v[116:119], v[76:79]\n"
    "v_fma_f32 v136, v132, 0.5, 1.0\n"
    "v_fma_f32 v137, v133, 0.5, 1.0\n"
    "v_mfma_f32_16x16x32_bf16 v[80:83], a[244:247], v[116:119], v[80:83]\n"
    "v_fma_f32 v138, v134, 0.5, 1.0\n"
    "v_fma_f32 v139, v135, 0.5, 1.0\n"
    "v_mfma_f32_16x16x32_bf16 v[84:87], a[224:227], v[116:119], v[84:87]\n"
    "ds_read_b128 v[116:119], v4 offset:50176\n"
    "v_fma_f32 v136, v132, v136, 1.0\n"
    "v_mfma_f32_16x16x32_bf16 v[76:79], a[240:243], v[96:99], v[76:79]\n"
    "v_fma_f32 v137, v133, v137, 1.0\n"
    "v_fma_f32 v138, v134, v138, 1.0\n"
    "v_fma_f32 v139, v135, v139, 1.0\n"
    "v_mfma_f32_16x16x32_bf16 v[80:83], v[152:155], v[96:99], v[80:83]\n"
    "v_rcp_f32 v136, v136\n"
    "v_mfma_f32_16x16x32_bf16 v[84:87], v[160:163], v[96:99], v[84:87]\n"
    "ds_read_b128 v[96:99], v4 offset:51200\n"
    "v_rcp_f32 v137, v137\n"
    "v_mfma_f32_16x16x32_bf16 v[76:79], v[164:167], v[100:103], v[76:79]\n"
    "v_rcp_f32 v138, v138\n"
    "v_mfma_f32_16x16x32_bf16 v[80:83], v[168:171], v[100:103], v[80:83]\n"
    "v_rcp_f32 v139, v139\n"
    "v_mfma_f32_16x16x32_bf16 v[84:87], v[172:175], v[100:103], v[84:87]\n"
    "ds_read_b128 v[100:103], v4 offset:52224\n"
    "v_fma_f32 v68, -v68, v136, v68\n"
    "v_fma_f32 v69, -v69, v137, v69\n"
    "s_waitcnt lgkmcnt(2)\n"
    "v_mfma_f32_16x16x32_bf16 v[76:79], v[176:179], v[104:107], v[76:79]\n"
    "v_fma_f32 v70, -v70, v138, v70\n"
    "v_fma_f32 v71, -v71, v139, v71\n"
    "v_mfma_f32_16x16x32_bf16 v[80:83], v[180:183], v[104:107], v[80:83]\n"
    "v_cvt_pk_bf16_f32 v124, v68, v69\n"
    "v_cvt_pk_bf16_f32 v125, v70, v71\n"
    "v_mfma_f32_16x16x32_bf16 v[84:87], v[184:187], v[104:107], v[84:87]\n"
    "v_mul_f32 v72, v72, v144\n"
    "v_mul_f32 v73, v73, v144\n"
    "v_mul_f32 v74, v74, v144\n"
    "v_mfma_f32_16x16x32_bf16 v[76:79], v[188:191], v[108:111], v[76:79]\n"
    "v_mul_f32 v75, v75, v144\n"
    "v_exp_f32 v132, v72\n"
    "v_mfma_f32_16x16x32_bf16 v[80:83], v[192:195], v[108:111], v[80:83]\n"
    "v_exp_f32 v133, v73\n"
    "v_mfma_f32_16x16x32_bf16 v[84:87], v[196:199], v[108:111], v[84:87]\n"
    "v_exp_f32 v134, v74\n"
    "v_mfma_f32_16x16x32_bf16 v[76:79], v[200:203], v[112:115], v[76:79]\n"
    "v_exp_f32 v135, v75\n"
    "v_mfma_f32_16x16x32_bf16 v[80:83], v[204:207], v[112:115], v[80:83]\n"
    "v_fma_f32 v136, v132, 0.5, 1.0\n"
    "v_fma_f32 v137, v133, 0.5, 1.0\n"
    "v_mfma_f32_16x16x32_bf16 v[84:87], v[208:211], v[112:115], v[84:87]\n"
    "v_fma_f32 v138, v134, 0.5, 1.0\n"
    "v_fma_f32 v139, v135, 0.5, 1.0\n"
    "v_mfma_f32_16x16x32_bf16 v[76:79], v[212:215], v[116:119], v[76:79]\n"
    "v_fma_f32 v136, v132, v136, 1.0\n"
    "v_fma_f32 v137, v133, v137, 1.0\n"
    "v_mfma_f32_16x16x32_bf16 v[80:83], v[216:219], v[116:119], v[80:83]\n"
    "v_fma_f32 v138, v134, v138, 1.0\n"
    "v_fma_f32 v139, v135, v139, 1.0\n"
    "v_mfma_f32_16x16x32_bf16 v[84:87], v[220:223], v[116:119], v[84:87]\n"
    "s_mov_b64 exec, 0xff\n"
    "s_waitcnt vmcnt(0)\n"
    "v_cmp_ne_u32 vcc, s98, v60\n"
    "s_mov_b64 exec, -1\n"
    "s_cbranch_vccz .Lpd13\n"
    "s_mov_b64 exec, 255\n"
    ".Lps13:\n"
    "s_sleep 1\n"
    "buffer_load_dword v60, v11, s[16:19], s82 offen sc1\n"
    "s_waitcnt vmcnt(0)\n"
    "v_cmp_ne_u32 vcc, s98, v60\n"
    "s_nop 1\n"
    "s_cbranch_vccz .Lpe13\n"
    "s_sub_u32 s67, s67, 1\n"
    "s_cbranch_scc0 .Lps13\n"
    "s_mov_b32 s67, 0\n"
    ".Lpe13:\n"
    "s_mov_b64 exec, -1\n"
    ".Lpd13:\n"
    "s_add_u32 m0, s53, 0\n"
    "s_add_u32 s87, s37, 0x1880000\n"
    "v_add_u32 v62, s87, v1\n"
    "global_load_lds_dwordx4 v62, s[16:17] sc1\n"
    "global_load_lds_dwordx4 v62, s[16:17] offset:1024 sc1\n"
    "global_load_lds_dwordx4 v62, s[16:17] offset:2048 sc1\n"
    "global_load_lds_dwordx4 v62, s[16:17] offset:3072 sc1\n"
    "v_rcp_f32 v136, v136\n"
    "s_waitcnt lgkmcnt(0)\n"
    "v_mfma_f32_16x16x32_bf16 v[76:79], v[224:227], v[96:99], v[76:79]\n"
    "v_rcp_f32 v137, v137\n"
    "v_mfma_f32_16x16x32_bf16 v[80:83], v[228:231], v[96:99], v[80:83]\n"
    "v_rcp_f32 v138, v138\n"
    "v_mfma_f32_16x16x32_bf16 v[84:87], v[232:235], v[96:99], v[84:87]\n"
    "v_rcp_f32 v139, v139\n"
    "v_mfma_f32_16x16x32_bf16 v[76:79], v[236:239], v[100:103], v[76:79]\n"
    "v_fma_f32 v72, -v72, v136, v72\n"
    "v_fma_f32 v73, -v73, v137, v73\n"
    "v_mfma_f32_16x16x32_bf16 v[80:83], v[240:243], v[100:103], v[80:83]\n"
    "v_fma_f32 v74, -v74, v138, v74\n"
    "v_fma_f32 v75, -v75, v139, v75\n"
    "v_mfma_f32_16x16x32_bf16 v[84:87], v[244:247], v[100:103], v[84:87]\n"
    "v_cvt_pk_bf16_f32 v128, v72, v73\n"
    "v_cvt_pk_bf16_f32 v129, v74, v75\n"
    "s_add_u32 m0, s53, 0x1000\n"
    "s_add_u32 s87, s37, 0x1881000\n"
    "v_add_u32 v62, s87, v1\n"
    "global_load_lds_dwordx4 v62, s[16:17] sc1\n"
    "global_load_lds_dwordx4 v62, s[16:17] offset:1024 sc1\n"
    "global_load_lds_dwordx4 v62, s[16:17] offset:2048 sc1\n"
    "global_load_lds_dwordx4 v62, s[16:17] offset:3072 sc1\n"
    "v_mul_f32 v76, v76, v145\n"
    "v_mul_f32 v77, v77, v145\n"
    "v_mul_f32 v78, v78, v145\n"
    "v_mul_f32 v79, v79, v145\n"
    "v_exp_f32 v132, v76\n"
    "v_exp_f32 v133, v77\n"
    "v_exp_f32 v134, v78\n"
    "v_exp_f32 v135, v79\n"
    "v_fma_f32 v136, v132, 0.5, 1.0\n"
    "v_fma_f32 v137, v133, 0.5, 1.0\n"
    "v_fma_f32 v138, v134, 0.5, 1.0\n"
    "v_fma_f32 v139, v135, 0.5, 1.0\n"
    "v_fma_f32 v136, v132, v136, 1.0\n"
    "v_fma_f32 v137, v133, v137, 1.0\n"
    "v_fma_f32 v138, v134, v138, 1.0\n"
    "v_fma_f32 v139, v135, v139, 1.0\n"
    "v_rcp_f32 v136, v136\n"
    "v_rcp_f32 v137, v137\n"
    "v_rcp_f32 v138, v138\n"
    "v_rcp_f32 v139, v139\n"
    "v_fma_f32 v76, -v76, v136, v76\n"
    "v_fma_f32 v77, -v77, v137, v77\n"
    "v_fma_f32 v78, -v78, v138, v78\n"
    "v_fma_f32 v79, -v79, v139, v79\n"
    "v_cvt_pk_bf16_f32 v122, v76, v77\n"
    "v_cvt_pk_bf16_f32 v123, v78, v79\n"
    "s_nop 0\n"
    "v_permlane16_swap_b32 v120, v122\n"
    "v_permlane16_swap_b32 v121, v123\n"
    "s_add_u32 s84, s45, 0x1896000\n"
    "buffer_store_dwordx4 v[120:123], v1, s[16:19], s84 offen sc1\n"
    "v_mul_f32 v80, v80, v145\n"
    "v_mul_f32 v81, v81, v145\n"
    "v_mul_f32 v82, v82, v145\n"
    "v_mul_f32 v83, v83, v145\n"
    "v_exp_f32 v132, v80\n"
    "v_exp_f32 v133, v81\n"
    "v_exp_f32 v134, v82\n"
    "v_exp_f32 v135, v83\n"
    "v_fma_f32 v136, v132, 0.5, 1.0\n"
    "v_fma_f32 v137, v133, 0.5, 1.0\n"
    "v_fma_f32 v138, v134, 0.5, 1.0\n"
    "v_fma_f32 v139, v135, 0.5, 1.0\n"
    "v_fma_f32 v136, v132, v136, 1.0\n"
    "v_fma_f32 v137, v133, v137, 1.0\n"
    "v_fma_f32 v138, v134, v138, 1.0\n"
    "v_fma_f32 v139, v135, v139, 1.0\n"
    "v_rcp_f32 v136, v136\n"
    "v_rcp_f32 v137, v137\n"
    "v_rcp_f32 v138, v138\n"
    "v_rcp_f32 v139, v139\n"
    "v_fma_f32 v80, -v80, v136, v80\n"
    "v_fma_f32 v81, -v81, v137, v81\n"
    "v_fma_f32 v82, -v82, v138, v82\n"
    "v_fma_f32 v83, -v83, v139, v83\n"
    "v_cvt_pk_bf16_f32 v126, v80, v81\n"
    "v_cvt_pk_bf16_f32 v127, v82, v83\n"
    "s_nop 0\n"
    "v_permlane16_swap_b32 v124, v126\n"
    "s_add_u32 m0, s53, 0x2000\n"
    "s_add_u32 s87, s37, 0x1882000\n"
    "v_add_u32 v62, s87, v1\n"
    "global_load_lds_dwordx4 v62, s[16:17] sc1\n"
    "global_load_lds_dwordx4 v62, s[16:17] offset:1024 sc1\n"
    "global_load_lds_dwordx4 v62, s[16:17] offset:2048 sc1\n"
    "global_load_lds_dwordx4 v62, s[16:17] offset:3072 sc1\n"
    "v_permlane16_swap_b32 v125, v127\n"
    "buffer_store_dwordx4 v[124:127], v1, s[16:19], s84 offen offset:1024 sc1\n"
    "v_mul_f32 v84, v84, v145\n"
    "v_mul_f32 v85, v85, v145\n"
    "v_mul_f32 v86, v86, v145\n"
    "v_mul_f32 v87, v87, v145\n"
    "v_exp_f32 v132, v84\n"
    "v_exp_f32 v133, v85\n"
    "v_exp_f32 v134, v86\n"
    "v_exp_f32 v135, v87\n"
    "v_fma_f32 v136, v132, 0.5, 1.0\n"
    "v_fma_f32 v137, v133, 0.5, 1.0\n"
    "v_fma_f32 v138, v134, 0.5, 1.0\n"
    "v_fma_f32 v139, v135, 0.5, 1.0\n"
    "v_fma_f32 v136, v132, v136, 1.0\n"
    "v_fma_f32 v137, v133, v137, 1.0\n"
    "v_fma_f32 v138, v134, v138, 1.0\n"
    "v_fma_f32 v139, v135, v139, 1.0\n"
    "v_rcp_f32 v136, v136\n"
    "v_rcp_f32 v137, v137\n"
    "v_rcp_f32 v138, v138\n"
    "v_rcp_f32 v139, v139\n"
    "v_fma_f32 v84, -v84, v136, v84\n"
    "v_fma_f32 v85, -v85, v137, v85\n"
    "v_fma_f32 v86, -v86, v138, v86\n"
    "global_load_dwordx4 a[160:163], v61, s[16:17] offset:-2048\n"
    "global_load_dwordx4 a[164:167], v61, s[16:17] offset:-1024\n"
    "v_fma_f32 v87, -v87, v139, v87\n"
    "v_cvt_pk_bf16_f32 v130, v84, v85\n"
    "v_cvt_pk_bf16_f32 v131, v86, v87\n"
    "s_nop 0\n"
    "v_permlane16_swap_b32 v128, v130\n"
    "v_permlane16_swap_b32 v129, v131\n"
    "buffer_store_dwordx4 v[128:131], v151, s[16:19], s84 offen offset:2048 sc1\n"
    "s_add_u32 m0, s53, 0x3000\n"
    "s_add_u32 s87, s37, 0x1883000\n"
    "v_add_u32 v62, s87, v1\n"
    "global_load_lds_dwordx4 v62, s[16:17] sc1\n"
    "global_load_lds_dwordx4 v62, s[16:17] offset:1024 sc1\n"
    "global_load_lds_dwordx4 v62, s[16:17] offset:2048 sc1\n"
    "global_load_lds_dwordx4 v62, s[16:17] offset:3072 sc1\n"
    "s_waitcnt vmcnt(17)\n"
    "ds_read_b128 v[96:99], v6 offset:0\n"
    "ds_read_b128 v[100:103], v6 offset:1024\n"
    "ds_read_b128 v[104:107], v6 offset:2048\n"
    "ds_read_b128 v[108:111], v6 offset:3072\n"
    "s_add_u32 s64, s57, 0x2400\n"
    "v_add_u32 v120, s64, v1\n"
    "s_add_u32 s64, s58, 0x1400\n"
    "v_add_u32 v121, s64, v1\n"
    "s_add_u32 s64, s59, 0x1c00\n"
    "v_add_u32 v122, s64, v1\n"
    "s_waitcnt lgkmcnt(2)\n"
    "v_mfma_f32_32x32x16_bf16 v[64:79], v[248:251], v[96:99], 0\n"
    "v_mfma_f32_32x32x16_bf16 v[80:95], v[252:255], v[96:99], 0\n"
    "v_mfma_f32_32x32x16_bf16 v[64:79], a[0:3], v[100:103], v[64:79]\n"
    "v_mfma_f32_32x32x16_bf16 v[80:95], a[4:7], v[100:103], v[80:95]\n"
    "s_add_u32 m0, s53, 0x4000\n"
    "s_add_u32 s87, s37, 0x1884000\n"
    "v_add_u32 v62, s87, v1\n"
    "global_load_lds_dwordx4 v62, s[16:17] sc1\n"
    "global_load_lds_dwordx4 v62, s[16:17] offset:1024 sc1\n"
    "global_load_lds_dwordx4 v62, s[16:17] offset:2048 sc1\n"
    "global_load_lds_dwordx4 v62, s[16:17] offset:3072 sc1\n"
    "s_waitcnt lgkmcnt(0)\n"
    "v_mfma_f32_32x32x16_bf16 v[64:79], a[8:11], v[104:107], v[64:79]\n"
    "s_waitcnt vmcnt(16)\n"
    "ds_read_b128 v[112:115], v6 offset:4096\n"
    "ds_read_b128 v[116:119], v6 offset:5120\n"
    "ds_read_b128 v[96:99], v6 offset:6144\n"
    "ds_read_b128 v[100:103], v6 offset:7168\n"
    "v_mfma_f32_32x32x16_bf16 v[80:95], a[12:15], v[104:107], v[80:95]\n"
    "v_mfma_f32_32x32x16_bf16 v[64:79], a[16:19], v[108:111], v[64:79]\n"
    "v_mfma_f32_32x32x16_bf16 v[80:95], a[20:23], v[108:111], v[80:95]\n"
    "s_waitcnt lgkmcnt(2)\n"
    "v_mfma_f32_32x32x16_bf16 v[64:79], a[24:27], v[112:115], v[64:79]\n"
    "v_mfma_f32_32x32x16_bf16 v[80:95], a[28:31], v[112:115], v[80:95]\n"
    "v_mfma_f32_32x32x16_bf16 v[64:79], a[32:35], v[116:119], v[64:79]\n"
    "v_mfma_f32_32x32x16_bf16 v[80:95], a[36:39], v[116:119], v[80:95]\n"
    "s_add_u32 m0, s53, 0x5000\n"
    "s_add_u32 s87, s37, 0x1885000\n"
    "v_add_u32 v62, s87, v1\n"
    "global_load_lds_dwordx4 v62, s[16:17] sc1\n"
    "global_load_lds_dwordx4 v62, s[16:17] offset:1024 sc1\n"
    "s_waitcnt lgkmcnt(0)\n"
    "v_mfma_f32_32x32x16_bf16 v[64:79], a[40:43], v[96:99], v[64:79]\n"
    "s_waitcnt vmcnt(13)\n"
    "ds_read_b128 v[104:107], v6 offset:8192\n"
    "v_mfma_f32_32x32x16_bf16 v[80:95], a[44:47], v[96:99], v[80:95]\n"
    "ds_read_b128 v[108:111], v6 offset:9216\n"
    "ds_read_b128 v[112:115], v6 offset:10240\n"
    "ds_read_b128 v[116:119], v6 offset:11264\n"
    "v_mfma_f32_32x32x16_bf16 v[64:79], a[48:51], v[100:103], v[64:79]\n"
    "v_mfma_f32_32x32x16_bf16 v[80:95], a[52:55], v[100:103], v[80:95]\n"
    "s_waitcnt lgkmcnt(3)\n"
    "v_mfma_f32_32x32x16_bf16 v[64:79], a[56:59], v[104:107], v[64:79]\n"
    "v_mfma_f32_32x32x16_bf16 v[80:95], a[60:63], v[104:107], v[80:95]\n"
    "s_waitcnt lgkmcnt(0)\n"
    "v_mfma_f32_32x32x16_bf16 v[64:79], a[64:67], v[108:111], v[64:79]\n"
    "s_waitcnt vmcnt(10)\n"
    "s_barrier\n"
    "v_mov_b32 v62, s98\n"
    "s_add_u32 s83, s47, 0x1f1c080\n"
    "s_mov_b64 exec, s[68:69]\n"
    "buffer_store_dword v62, off, s[16:19], s83 sc1\n"
    "s_mov_b64 exec, -1\n"
    "v_mfma_f32_32x32x16_bf16 v[80:95], a[68:71], v[108:111], v[80:95]\n"
    "v_mfma_f32_32x32x16_bf16 v[64:79], a[72:75], v[112:115], v[64:79]\n"
    "s_waitcnt vmcnt(7)\n"
    "ds_read_b128 v[96:99], v6 offset:12288\n"
    "v_mfma_f32_32x32x16_bf16 v[80:95], a[76:79], v[112:115], v[80:95]\n"
    "ds_read_b128 v[100:103], v6 offset:13312\n"
    "ds_read_b128 v[104:107], v6 offset:14336\n"
    "ds_read_b128 v[108:111], v6 offset:15360\n"
    "v_mfma_f32_32x32x16_bf16 v[64:79], a[80:83], v[116:119], v[64:79]\n"
    "v_mfma_f32_32x32x16_bf16 v[80:95], a[84:87], v[116:119], v[80:95]\n"
    "s_waitcnt lgkmcnt(3)\n"
    "v_mfma_f32_32x32x16_bf16 v[64:79], a[88:91], v[96:99], v[64:79]\n"
    "v_mfma_f32_32x32x16_bf16 v[80:95], a[92:95], v[96:99], v[80:95]\n"
    "s_waitcnt lgkmcnt(0)\n"
    "v_mfma_f32_32x32x16_bf16 v[64:79], a[96:99], v[100:103], v[64:79]\n"
    "v_mfma_f32_32x32x16_bf16 v[80:95], a[100:103], v[100:103], v[80:95]\n"
    "v_mfma_f32_32x32x16_bf16 v[64:79], a[104:107], v[104:107], v[64:79]\n"
    "s_waitcnt vmcnt(3)\n"
    "ds_read_b128 v[112:115], v6 offset:16384\n"
    "v_mfma_f32_32x32x16_bf16 v[80:95], a[108:111], v[104:107], v[80:95]\n"
    "ds_read_b128 v[116:119], v6 offset:17408\n"
    "ds_read_b128 v[96:99], v6 offset:18432\n"
    "ds_read_b128 v[100:103], v6 offset:19456\n"
    "v_mfma_f32_32x32x16_bf16 v[64:79], a[112:115], v[108:111], v[64:79]\n"
    "v_mfma_f32_32x32x16_bf16 v[80:95], a[116:119], v[108:111], v[80:95]\n"
    "s_mov_b32 s82, 0x1f1c080\n"
    "s_mov_b64 exec, 0xff\n"
    "buffer_load_dword v60, v11, s[16:19], s82 offen sc1\n"
    "s_mov_b64 exec, -1\n"
    "s_waitcnt lgkmcnt(3)\n"
    "v_mfma_f32_32x32x16_bf16 v[80:95], a[124:127], v[112:115], v[80:95]\n"
    "s_waitcnt vmcnt(2)\n"
    "ds_read_b128 v[104:107], v6 offset:20480\n"
    "s_waitcnt lgkmcnt(1)\n"
    "v_mfma_f32_32x32x16_bf16 v[80:95], a[140:143], v[116:119], v[80:95]\n"
    "ds_read_b128 v[108:111], v6 offset:21504\n"
    "v_mfma_f32_32x32x16_bf16 v[80:95], a[156:159], v[96:99], v[80:95]\n"
    "v_mfma_f32_32x32x16_bf16 v[80:95], a[136:139], v[100:103], v[80:95]\n"
    "s_waitcnt lgkmcnt(1)\n"
    "v_mfma_f32_32x32x16_bf16 v[80:95], a[152:155], v[104:107], v[80:95]\n"
    "s_waitcnt lgkmcnt(0)\n"
    "v_mfma_f32_32x32x16_bf16 v[80:95], a[164:167], v[108:111], v[80:95]\n"
    "v_mfma_f32_32x32x16_bf16 v[64:79], a[120:123], v[112:115], v[64:79]\n"
    "s_nop 3\n"
    "ds_write_b128 v6, v[80:83] offset:5120\n"
    "v_mfma_f32_32x32x16_bf16 v[64:79], a[132:135], v[116:119], v[64:79]\n"
    "v_mfma_f32_32x32x16_bf16 v[64:79], a[148:151], v[96:99], v[64:79]\n"
    "ds_write_b128 v6, v[84:87] offset:6144\n"
    "v_mfma_f32_32x32x16_bf16 v[64:79], a[128:131], v[100:103], v[64:79]\n"
    "ds_write_b128 v6, v[88:91] offset:7168\n"
    "v_mfma_f32_32x32x16_bf16 v[64:79], a[144:147], v[104:107], v[64:79]\n"
    "v_mfma_f32_32x32x16_bf16 v[64:79], a[160:163], v[108:111], v[64:79]\n"
    "ds_write_b128 v6, v[92:95] offset:8192\n"
    "s_nop 7\n"
    "s_nop 2\n"
    "ds_write_b128 v6, v[72:75] offset:9216\n"
    "ds_write_b128 v6, v[76:79] offset:10240\n"
    "v_fma_f32 v16, v64, s77, v16\n"
    "v_fma_f32 v17, v65, s77, v17\n"
    "v_fma_f32 v18, v66, s77, v18\n"
    "v_fma_f32 v19, v67, s77, v19\n"
    "v_fma_f32 v20, v68, s77, v20\n"
    "v_fma_f32 v21, v69, s77, v21\n"
    "v_fma_f32 v22, v70, s77, v22\n"
    "v_fma_f32 v23, v71, s77, v23\n"
    "s_waitcnt lgkmcnt(0)\n"
    "s_barrier\n"
    "ds_read_b128 v[104:107], v121 offset:0\n"
    "ds_read_b128 v[108:111], v121 offset:1024\n"
    "ds_read_b128 v[112:115], v122 offset:0\n"
    "ds_read_b128 v[116:119], v122 offset:1024\n"
    "ds_read_b128 v[96:99], v120 offset:0\n"
    "ds_read_b128 v[100:103], v120 offset:1024\n"
    "s_waitcnt lgkmcnt(4)\n"
    "v_fma_f32 v16, v104, s77, v16\n"
    "v_fma_f32 v17, v105, s77, v17\n"
    "v_fma_f32 v18, v106, s77, v18\n"
    "v_fma_f32 v19, v107, s77, v19\n"
    "v_fma_f32 v20, v108, s77, v20\n"
    "v_fma_f32 v21, v109, s77, v21\n"
    "v_fma_f32 v22, v110, s77, v22\n"
    "v_fma_f32 v23, v111, s77, v23\n"
    "s_waitcnt lgkmcnt(0)\n"
    "v_fma_f32 v16, v112, s77, v16\n"
    "v_fma_f32 v17, v113, s77, v17\n"
    "v_fma_f32 v18, v114, s77, v18\n"
    "v_fma_f32 v19, v115, s77, v19\n"
    "v_fma_f32 v20, v116, s77, v20\n"
    "v_fma_f32 v21, v117, s77, v21\n"
    "v_fma_f32 v22, v118, s77, v22\n"
    "v_fma_f32 v23, v119, s77, v23\n"
    "v_fma_f32 v16, v96, s77, v16\n"
    "v_fma_f32 v17, v97, s77, v17\n"
    "v_fma_f32 v18, v98, s77, v18\n"
    "v_fma_f32 v19, v99, s77, v19\n"
    "v_fma_f32 v20, v100, s77, v20\n"
    "v_fma_f32 v21, v101, s77, v21\n"
    "v_fma_f32 v22, v102, s77, v22\n"
    "v_fma_f32 v23, v103, s77, v23\n"
    "v_cvt_pk_bf16_f32 v124, v16, v17\n"
    "v_cvt_pk_bf16_f32 v125, v18, v19\n"
    "v_cvt_pk_bf16_f32 v126, v20, v21\n"
    "v_cvt_pk_bf16_f32 v127, v22, v23\n"
    "s_add_u32 s84, s46, 0\n"
    "buffer_store_dwordx4 v[124:127], v10, s[20:23], s84 offen\n"
    "s_mov_b64 exec, 0xff\n"
    "s_waitcnt vmcnt(1)\n"
    "v_cmp_ne_u32 vcc, s98, v60\n"
    "s_mov_b64 exec, -1\n"
    "s_cbranch_vccz .Lpd14\n"
    "s_mov_b64 exec, 255\n"
    ".Lps14:\n"
    "s_sleep 1\n"
    "buffer_load_dword v60, v11, s[16:19], s82 offen sc1\n"
    "s_waitcnt vmcnt(0)\n"
    "v_cmp_ne_u32 vcc, s98, v60\n"
    "s_nop 1\n"
    "s_cbranch_vccz .Lpe14\n"
    "s_sub_u32 s67, s67, 1\n"
    "s_cbranch_scc0 .Lps14\n"
    "s_mov_b32 s67, 0\n"
    ".Lpe14:\n"
    "s_mov_b64 exec, -1\n"
    ".Lpd14:\n"
    "s_add_u32 m0, s52, 0x16000\n"
    "s_add_u32 s87, s37, 0x1896000\n"
    "v_add_u32 v62, s87, v1\n"
    "global_load_lds_dwordx4 v62, s[16:17] sc1\n"
    "global_load_lds_dwordx4 v62, s[16:17] offset:1024 sc1\n"
    "global_load_lds_dwordx4 v62, s[16:17] offset:2048 sc1\n"
    "global_load_lds_dwordx4 v62, s[16:17] offset:3072 sc1\n"
    "s_add_u32 m0, s52, 0x17000\n"
    "s_add_u32 s87, s37, 0x1897000\n"
    "v_add_u32 v62, s87, v1\n"
    "global_load_lds_dwordx4 v62, s[16:17] sc1\n"
    "global_load_lds_dwordx4 v62, s[16:17] offset:1024 sc1\n"
    "global_load_lds_dwordx4 v62, s[16:17] offset:2048 sc1\n"
    "global_load_lds_dwordx4 v62, s[16:17] offset:3072 sc1\n"
    "s_add_u32 m0, s52, 0x18000\n"
    "s_add_u32 s87, s37, 0x1898000\n"
    "v_add_u32 v62, s87, v1\n"
    "global_load_lds_dwordx4 v62, s[16:17] sc1\n"
    "global_load_lds_dwordx4 v62, s[16:17] offset:1024 sc1\n"
    "global_load_lds_dwordx4 v62, s[16:17] offset:2048 sc1\n"
    "global_load_lds_dwordx4 v62, s[16:17] offset:3072 sc1\n"
    "s_add_u32 m0, s52, 0x19000\n"
    "s_add_u32 s87, s37, 0x1899000\n"
    "v_add_u32 v62, s87, v1\n"
    "global_load_lds_dwordx4 v62, s[16:17] sc1\n"
    "global_load_lds_dwordx4 v62, s[16:17] offset:1024 sc1\n"
    "global_load_lds_dwordx4 v62, s[16:17] offset:2048 sc1\n"
    "global_load_lds_dwordx4 v62, s[16:17] offset:3072 sc1\n"
    "s_add_u32 m0, s52, 0x1a000\n"
    "s_add_u32 s87, s37, 0x189a000\n"
    "v_add_u32 v62, s87, v1\n"
    "global_load_lds_dwordx4 v62, s[16:17] sc1\n"
    "s_waitcnt vmcnt(13)\n"
    "ds_read_b128 v[96:99], v7 offset:0\n"
    "ds_read_b128 v[100:103], v7 offset:1024\n"
    "ds_read_b128 v[104:107], v7 offset:2048\n"
    "ds_read_b128 v[108:111], v7 offset:3072\n"
    "s_add_u32 m0, s53, 0\n"
    "s_add_u32 s87, s37, 0x189a400\n"
    "v_add_u32 v62, s87, v1\n"
    "global_load_lds_dwordx4 v62, s[16:17] sc1\n"
    "global_load_lds_dwordx4 v62, s[16:17] offset:1024 sc1\n"
    "global_load_lds_dwordx4 v62, s[16:17] offset:2048 sc1\n"
    "global_load_lds_dwordx4 v62, s[16:17] offset:3072 sc1\n"
    "s_add_u32 s64, s60, 0x17000\n"
    "v_add_u32 v120, s64, v1\n"
    "s_add_u32 s64, s61, 0x16000\n"
    "v_add_u32 v121, s64, v1\n"
    "s_add_u32 s64, s62, 0x16800\n"
    "v_add_u32 v122, s64, v1\n"
    "s_waitcnt lgkmcnt(2)\n"
    "v_mfma_f32_32x32x16_bf16 v[64:79], v[248:251], v[96:99], 0\n"
    "v_mfma_f32_32x32x16_bf16 v[80:95], v[252:255], v[96:99], 0\n"
    "v_mfma_f32_32x32x16_bf16 v[64:79], a[0:3], v[100:103], v[64:79]\n"
    "v_mfma_f32_32x32x16_bf16 v[80:95], a[4:7], v[100:103], v[80:95]\n"
    "s_waitcnt lgkmcnt(0)\n"
    "v_mfma_f32_32x32x16_bf16 v[64:79], a[8:11], v[104:107], v[64:79]\n"
    "s_waitcnt vmcnt(13)\n"
    "ds_read_b128 v[112:115], v7 offset:4096\n"
    "s_add_u32 m0, s53, 0x1000\n"
    "s_add_u32 s87, s37, 0x189b400\n"
    "v_add_u32 v62, s87, v1\n"
    "global_load_lds_dwordx4 v62, s[16:17] sc1\n"
    "v_mfma_f32_32x32x16_bf16 v[80:95], a[12:15], v[104:107], v[80:95]\n"
    "ds_read_b128 v[116:119], v7 offset:5120\n"
    "ds_read_b128 v[96:99], v7 offset:6144\n"
    "ds_read_b128 v[100:103], v7 offset:7168\n"
    "v_mfma_f32_32x32x16_bf16 v[64:79], a[16:19], v[108:111], v[64:79]\n"
    "v_mfma_f32_32x32x16_bf16 v[80:95], a[20:23], v[108:111], v[80:95]\n"
    "s_waitcnt lgkmcnt(3)\n"
    "v_mfma_f32_32x32x16_bf16 v[64:79], a[24:27], v[112:115], v[64:79]\n"
    "v_mfma_f32_32x32x16_bf16 v[80:95], a[28:31], v[112:115], v[80:95]\n"
    "s_waitcnt lgkmcnt(0)\n"
    "v_mfma_f32_32x32x16_bf16 v[64:79], a[32:35], v[116:119], v[64:79]\n"
    "v_mfma_f32_32x32x16_bf16 v[80:95], a[36:39], v[116:119], v[80:95]\n"
    "v_mfma_f32_32x32x16_bf16 v[64:79], a[40:43], v[96:99], v[64:79]\n"
    "s_waitcnt vmcnt(10)\n"
    "ds_read_b128 v[104:107], v7 offset:8192\n"
    "v_mfma_f32_32x32x16_bf16 v[80:95], a[44:47], v[96:99], v[80:95]\n"
    "ds_read_b128 v[108:111], v7 offset:9216\n"
    "ds_read_b128 v[112:115], v7 offset:10240\n"
    "ds_read_b128 v[116:119], v7 offset:11264\n"
    "v_mfma_f32_32x32x16_bf16 v[64:79], a[48:51], v[100:103], v[64:79]\n"
    "v_mfma_f32_32x32x16_bf16 v[80:95], a[52:55], v[100:103], v[80:95]\n"
    "s_waitcnt lgkmcnt(3)\n"
    "v_mfma_f32_32x32x16_bf16 v[64:79], a[56:59], v[104:107], v[64:79]\n"
    "v_mfma_f32_32x32x16_bf16 v[80:95], a[60:63], v[104:107], v[80:95]\n"
    "s_waitcnt lgkmcnt(0)\n"
    "v_mfma_f32_32x32x16_bf16 v[64:79], a[64:67], v[108:111], v[64:79]\n"
    "v_mfma_f32_32x32x16_bf16 v[80:95], a[68:71], v[108:111], v[80:95]\n"
    "v_mfma_f32_32x32x16_bf16 v[64:79], a[72:75], v[112:115], v[64:79]\n"
    "s_waitcnt vmcnt(6)\n"
    "ds_read_b128 v[96:99], v7 offset:12288\n"
    "v_mfma_f32_32x32x16_bf16 v[80:95], a[76:79], v[112:115], v[80:95]\n"
    "ds_read_b128 v[100:103], v7 offset:13312\n"
    "ds_read_b128 v[104:107], v7 offset:14336\n"
    "ds_read_b128 v[108:111], v7 offset:15360\n"
    "v_mfma_f32_32x32x16_bf16 v[64:79], a[80:83], v[116:119], v[64:79]\n"
    "v_mfma_f32_32x32x16_bf16 v[80:95], a[84:87], v[116:119], v[80:95]\n"
    "s_waitcnt lgkmcnt(3)\n"
    "v_mfma_f32_32x32x16_bf16 v[64:79], a[88:91], v[96:99], v[64:79]\n"
    "v_mfma_f32_32x32x16_bf16 v[80:95], a[92:95], v[96:99], v[80:95]\n"
    "s_waitcnt lgkmcnt(0)\n"
    "v_mfma_f32_32x32x16_bf16 v[64:79], a[96:99], v[100:103], v[64:79]\n"
    "v_mfma_f32_32x32x16_bf16 v[80:95], a[100:103], v[100:103], v[80:95]\n"
    "v_mfma_f32_32x32x16_bf16 v[64:79], a[104:107], v[104:107], v[64:79]\n"
    "s_waitcnt vmcnt(2)\n"
    "ds_read_b128 v[112:115], v7 offset:16384\n"
    "v_mfma_f32_32x32x16_bf16 v[80:95], a[108:111], v[104:107], v[80:95]\n"
    "ds_read_b128 v[116:119], v6 offset:0\n"
    "ds_read_b128 v[96:99], v6 offset:1024\n"
    "ds_read_b128 v[100:103], v6 offset:2048\n"
    "v_mfma_f32_32x32x16_bf16 v[64:79], a[112:115], v[108:111], v[64:79]\n"
    "v_mfma_f32_32x32x16_bf16 v[80:95], a[116:119], v[108:111], v[80:95]\n"
    "s_waitcnt lgkmcnt(3)\n"
    "v_mfma_f32_32x32x16_bf16 v[80:95], a[124:127], v[112:115], v[80:95]\n"
    "s_waitcnt vmcnt(0)\n"
    "ds_read_b128 v[104:107], v6 offset:3072\n"
    "s_waitcnt lgkmcnt(1)\n"
    "v_mfma_f32_32x32x16_bf16 v[80:95], a[140:143], v[116:119], v[80:95]\n"
    "ds_read_b128 v[108:111], v6 offset:4096\n"
    "v_mfma_f32_32x32x16_bf16 v[80:95], a[156:159], v[96:99], v[80:95]\n"
    "v_mfma_f32_32x32x16_bf16 v[80:95], a[136:139], v[100:103], v[80:95]\n"
    "s_waitcnt lgkmcnt(1)\n"
    "v_mfma_f32_32x32x16_bf16 v[80:95], a[152:155], v[104:107], v[80:95]\n"
    "s_waitcnt lgkmcnt(0)\n"
    "v_mfma_f32_32x32x16_bf16 v[80:95], a[164:167], v[108:111], v[80:95]\n"
    "v_mfma_f32_32x32x16_bf16 v[64:79], a[120:123], v[112:115], v[64:79]\n"
    "s_nop 3\n"
    "ds_write_b128 v7, v[80:83] offset:0\n"
    "v_mfma_f32_32x32x16_bf16 v[64:79], a[132:135], v[116:119], v[64:79]\n"
    "v_mfma_f32_32x32x16_bf16 v[64:79], a[148:151], v[96:99], v[64:79]\n"
    "ds_write_b128 v7, v[84:87] offset:1024\n"
    "v_mfma_f32_32x32x16_bf16 v[64:79], a[128:131], v[100:103], v[64:79]\n"
    "ds_write_b128 v7, v[88:91] offset:2048\n"
    "v_mfma_f32_32x32x16_bf16 v[64:79], a[144:147], v[104:107], v[64:79]\n"
    "v_mfma_f32_32x32x16_bf16 v[64:79], a[160:163], v[108:111], v[64:79]\n"
    "ds_write_b128 v7, v[92:95] offset:3072\n"
    "s_nop 7\n"
    "s_nop 2\n"
    "ds_write_b128 v7, v[72:75] offset:4096\n"
    "ds_write_b128 v7, v[76:79] offset:5120\n"
    "v_fma_f32 v24, v64, s77, v24\n"
    "v_fma_f32 v25, v65, s77, v25\n"
    "v_fma_f32 v26, v66, s77, v26\n"
    "v_fma_f32 v27, v67, s77, v27\n"
    "v_fma_f32 v28, v68, s77, v28\n"
    "v_fma_f32 v29, v69, s77, v29\n"
    "v_fma_f32 v30, v70, s77, v30\n"
    "v_fma_f32 v31, v71, s77, v31\n"
    "s_waitcnt lgkmcnt(0)\n"
    "s_barrier\n"
    "ds_read_b128 v[104:107], v121 offset:0\n"
    "ds_read_b128 v[108:111], v121 offset:1024\n"
    "ds_read_b128 v[112:115], v122 offset:0\n"
    "ds_read_b128 v[116:119], v122 offset:1024\n"
    "ds_read_b128 v[96:99], v120 offset:0\n"
    "ds_read_b128 v[100:103], v120 offset:1024\n"
    "s_waitcnt lgkmcnt(4)\n"
    "v_fma_f32 v24, v104, s77, v24\n"
    "v_fma_f32 v25, v105, s77, v25\n"
    "v_fma_f32 v26, v106, s77, v26\n"
    "v_fma_f32 v27, v107, s77, v27\n"
    "v_fma_f32 v28, v108, s77, v28\n"
    "v_fma_f32 v29, v109, s77, v29\n"
    "v_fma_f32 v30, v110, s77, v30\n"
    "v_fma_f32 v31, v111, s77, v31\n"
    "s_waitcnt lgkmcnt(0)\n"
    "v_fma_f32 v24, v112, s77, v24\n"
    "v_fma_f32 v25, v113, s77, v25\n"
    "v_fma_f32 v26, v114, s77, v26\n"
    "v_fma_f32 v27, v115, s77, v27\n"
    "v_fma_f32 v28, v116, s77, v28\n"
    "v_fma_f32 v29, v117, s77, v29\n"
    "v_fma_f32 v30, v118, s77, v30\n"
    "v_fma_f32 v31, v119, s77, v31\n"
    "v_fma_f32 v24, v96, s77, v24\n"
    "v_fma_f32 v25, v97, s77, v25\n"
    "v_fma_f32 v26, v98, s77, v26\n"
    "v_fma_f32 v27, v99, s77, v27\n"
    "v_fma_f32 v28, v100, s77, v28\n"
    "v_fma_f32 v29, v101, s77, v29\n"
    "v_fma_f32 v30, v102, s77, v30\n"
    "v_fma_f32 v31, v103, s77, v31\n"
    "v_cvt_pk_bf16_f32 v124, v24, v25\n"
    "v_cvt_pk_bf16_f32 v125, v26, v27\n"
    "v_cvt_pk_bf16_f32 v126, v28, v29\n"
    "v_cvt_pk_bf16_f32 v127, v30, v31\n"
    "s_add_u32 s84, s46, 0x400\n"
    "buffer_store_dwordx4 v[124:127], v10, s[20:23], s84 offen\n"
    "s_waitcnt vmcnt(0)\n"
    "s_or_b32 s63, s30, s29\n"
    "s_or_b32 s63, s63, s28\n"
    "s_cmp_eq_u32 s63, 0\n"
    "s_cbranch_scc0 .LBend\n"
    "s_mov_b32 s64, 32653312\n"
    ".LBs:\n"
    "buffer_load_dwordx4 v[4:7], v1, s[16:19], s64 offen sc1\n"
    "s_waitcnt vmcnt(0)\n"
    "v_xor_b32 v4, s98, v4\n"
    "v_xor_b32 v5, s98, v5\n"
    "v_xor_b32 v6, s98, v6\n"
    "v_xor_b32 v7, s98, v7\n"
    "v_or_b32 v4, v4, v5\n"
    "v_or_b32 v6, v6, v7\n"
    "v_or_b32 v4, v4, v6\n"
    "v_cmp_ne_u32 vcc, 0, v4\n"
    "s_nop 1\n"
    "s_cbranch_vccz .LBe\n"
    "s_sub_u32 s67, s67, 1\n"
    "s_cbranch_scc0 .LBs\n"
    ".LBe:\n"
    "s_mov_b32 s64, 32636928\n"
    "v_mov_b32 v4, s98\n"
    "s_mov_b64 exec, 1\n"
    "buffer_store_dword v4, off, s[16:19], s64 sc0 sc1\n"
    "s_mov_b64 exec, -1\n"
    ".LBend:\n"
    "s_waitcnt vmcnt(0) lgkmcnt(0)\n"
    "MAINEND:\n"
    : "+{v[192:195]}"(ob[0]), "+{v[196:199]}"(ob[1]), "+{v[200:203]}"(ob[2]), "+{v[204:207]}"(ob[3]), "+{v208}"(fb[0]), "+{v209}"(fb[1]), "+{v210}"(fb[2]), "+{v211}"(fb[3])
    : [ka] "s"(__builtin_amdgcn_kernarg_segment_ptr()), [tid] "v"(threadIdx.x), [bid] "s"(blockIdx.x), [ldsb] "s"(ldsb), [tok] "s"(tok), [offB] "s"(offB)
    : "memory", "vcc", "scc", "s16", "s17", "s18", "s19", "s20", "s21", "s22", "s23", "s24", "s25", "s26", "s27", "s28", "s29", "s30", "s31", "s34", "s35", "s36", "s37", "s38", "s39", "s40", "s41", "s42", "s43", "s44", "s45", "s46", "s47", "s48", "s49", "s50", "s51", "s52", "s53", "s54", "s55", "s56", "s57", "s58", "s59", "s60", "s61", "s62", "s63", "s64", "s65", "s66", "s67", "s68", "s69", "s70", "s71", "s72", "s73", "s74", "s75", "s76", "s77", "s78", "s79", "s80", "s81", "s82", "s83", "s84", "s85", "s86", "s87", "s88", "s89", "s90", "s91", "s92", "s93", "s94", "s95", "s96", "s97", "s98", "s99", "s100", "s101", "v1", "v2", "v3", "v4", "v5", "v6", "v7", "v8", "v9", "v10", "v11", "v12", "v13", "v14", "v15", "v16", "v17", "v18", "v19", "v20", "v21", "v22", "v23", "v24", "v25", "v26", "v27", "v28", "v29", "v30", "v31", "v32", "v33", "v34", "v35", "v36", "v37", "v38", "v39", "v40", "v41", "v42", "v43", "v44", "v45", "v46", "v47", "v48", "v49", "v50", "v51", "v52", "v53", "v54", "v55", "v56", "v57", "v58", "v59", "v60", "v61", "v62", "v63", "v64", "v65", "v66", "v67", "v68", "v69", "v70", "v71", "v72", "v73", "v74", "v75", "v76", "v77", "v78", "v79", "v80", "v81", "v82", "v83", "v84", "v85", "v86", "v87", "v88", "v89", "v90", "v91", "v92", "v93", "v94", "v95", "v96", "v97", "v98", "v99", "v100", "v101", "v102", "v103", "v104", "v105", "v106", "v107", "v108", "v109", "v110", "v111", "v112", "v113", "v114", "v115", "v116", "v117", "v118", "v119", "v120", "v121", "v122", "v123", "v124", "v125", "v126", "v127", "v128", "v129", "v130", "v131", "v132", "v133", "v134", "v135", "v136", "v137", "v138", "v139", "v140", "v141", "v142", "v143", "v144", "v145", "v146", "v147", "v148", "v149", "v150", "v151", "v152", "v153", "v154", "v155", "v156", "v157", "v158", "v159", "v160", "v161", "v162", "v163", "v164", "v165", "v166", "v167", "v168", "v169", "v170", "v171", "v172", "v173", "v174", "v175", "v176", "v177", "v178", "v179", "v180", "v181", "v182", "v183", "v184", "v185", "v186", "v187", "v188", "v189", "v190", "v191", "v212", "v213", "v214", "v215", "v216", "v217", "v218", "v219", "v220", "v221", "v222", "v223", "v224", "v225", "v226", "v227", "v228", "v229", "v230", "v231", "v232", "v233", "v234", "v235", "v236", "v237", "v238", "v239", "v240", "v241", "v242", "v243", "v244", "v245", "v246", "v247", "v248", "v249", "v250", "v251", "v252", "v253", "v254", "v255", "a0", "a1", "a2", "a3", "a4", "a5", "a6", "a7", "a8", "a9", "a10", "a11", "a12", "a13", "a14", "a15", "a16", "a17", "a18", "a19", "a20", "a21", "a22", "a23", "a24", "a25", "a26", "a27", "a28", "a29", "a30", "a31", "a32", "a33", "a34", "a35", "a36", "a37", "a38", "a39", "a40", "a41", "a42", "a43", "a44", "a45", "a46", "a47", "a48", "a49", "a50", "a51", "a52", "a53", "a54", "a55", "a56", "a57", "a58", "a59", "a60", "a61", "a62", "a63", "a64", "a65", "a66", "a67", "a68", "a69", "a70", "a71", "a72", "a73", "a74", "a75", "a76", "a77", "a78", "a79", "a80", "a81", "a82", "a83", "a84", "a85", "a86", "a87", "a88", "a89", "a90", "a91", "a92", "a93", "a94", "a95", "a96", "a97", "a98", "a99", "a100", "a101", "a102", "a103", "a104", "a105", "a106", "a107", "a108", "a109", "a110", "a111", "a112", "a113", "a114", "a115", "a116", "a117", "a118", "a119", "a120", "a121", "a122", "a123", "a124", "a125", "a126", "a127", "a128", "a129", "a130", "a131", "a132", "a133", "a134", "a135", "a136", "a137", "a138", "a139", "a140", "a141", "a142", "a143", "a144", "a145", "a146", "a147", "a148", "a149", "a150", "a151", "a152", "a153", "a154", "a155", "a156", "a157", "a158", "a159", "a160", "a161", "a162", "a163", "a164", "a165", "a166", "a167", "a168", "a169", "a170", "a171", "a172", "a173", "a174", "a175", "a176", "a177", "a178", "a179", "a180", "a181", "a182", "a183", "a184", "a185", "a186", "a187", "a188", "a189", "a190", "a191", "a192", "a193", "a194", "a195", "a196", "a197", "a198", "a199", "a200", "a201", "a202", "a203", "a204", "a205", "a206", "a207", "a208", "a209", "a210", "a211", "a212", "a213", "a214", "a215", "a216", "a217", "a218", "a219", "a220", "a221", "a222", "a223", "a224", "a225", "a226", "a227", "a228", "a229", "a230", "a231", "a232", "a233", "a234", "a235", "a236", "a237", "a238", "a239", "a240", "a241", "a242", "a243", "a244", "a245", "a246", "a247", "a248", "a249", "a250", "a251", "a252", "a253", "a254", "a255");
}

extern "C" void kernel_launch(void* const* d_in, const int* in_sizes, int n_in, void* d_out, int out_size, void* d_ws, size_t ws_size,
                              hipStream_t stream) {
  main_kernel<<<256, 256, 0, stream>>>((const u16*)d_in[0], (const u16*)d_in[1], (const u16*)d_in[2], (const u16*)d_in[4], (const u16*)d_in[6],
                                       (const u16*)d_in[5], (unsigned char*)d_ws, (unsigned short*)d_out);
}
```
